```python
import torch

def _softmax(x, axis=-1):
    return torch.softmax(x, dim=axis)
def _gelu(x):
    return torch.nn.functional.gelu(x, approximate="tanh")
def _silu(x):
    return torch.nn.functional.silu(x)
def _sigmoid(x):
    return torch.sigmoid(x)
def _layernorm(x, axis=-1, eps=1e-5):
    m = x.mean(dim=axis, keepdim=True)
    v = x.var(dim=axis, keepdim=True, unbiased=False)
    return (x - m) / torch.sqrt(v + eps)
def _scatter_add(tgt, idx, src, axis=0):
    return tgt.clone().index_add_(axis, idx, src)
def _segment_sum(src, seg, n):
    out = torch.zeros((n,) + src.shape[1:], dtype=src.dtype)
    return out.index_add_(0, seg, src)
def _pad(x, pw):
    flat = tuple(v for pair in reversed(pw) for v in pair)
    return torch.nn.functional.pad(x, flat)

kernel_name = 'standalone__mha_block_cached__near_ridge'

def setup_inputs():

    _w = (lambda a: (lambda t: t.to(torch.bfloat16) if t.is_floating_point() else t)(torch.as_tensor(a)))
    B, Sq, D = 8, 128, 576
    H, Sk, Dh = 32, 1024, 128
    return {
        "x": _w((lambda *s: torch.randn(*s, dtype=torch.bfloat16) * 0.8)(B, Sq, D)),
        "k_cache": _w((lambda *s: torch.randn(*s, dtype=torch.bfloat16) * 0.8)(B, H, Sk, Dh)),
        "v_cache": _w((lambda *s: torch.randn(*s, dtype=torch.bfloat16) * 0.05)(B, H, Sk, Dh)),
        "wq": _w((lambda *s: torch.randn(*s, dtype=torch.bfloat16) * 0.8)(D, H * Dh) * (D ** -0.5)),
        "wo": _w((lambda *s: torch.randn(*s, dtype=torch.bfloat16) * 0.05)(H * Dh, D) * ((H * Dh) ** -0.5)),
    }

def reference(x, k_cache, v_cache, wq, wo):
    B, Sq, D = x.shape
    H, Dh = 32, 128
    q = (lambda x, *d: x.permute(*d))((x @ wq).reshape(B, Sq, H, Dh), 0, 2, 1, 3)
    scores = torch.einsum("bhqd,bhkd->bhqk", q, k_cache) * (Dh ** -0.5)
    p = _softmax(scores, axis=-1)
    o = torch.einsum("bhqk,bhkd->bhqd", p, v_cache)
    return (lambda x, *d: x.permute(*d))(o, 0, 2, 1, 3).reshape(B, Sq, H * Dh) @ wo
```

```cpp
#include <hip/hip_runtime.h>
#include <hip/hip_bf16.h>


typedef short    bf16x8 __attribute__((ext_vector_type(8)));
typedef short    s16x4  __attribute__((ext_vector_type(4)));
typedef float    f32x16 __attribute__((ext_vector_type(16)));
typedef float    f32x2  __attribute__((ext_vector_type(2)));
typedef __bf16   bf16x2 __attribute__((ext_vector_type(2)));
typedef unsigned u32x4  __attribute__((ext_vector_type(4)));
typedef unsigned u32x2  __attribute__((ext_vector_type(2)));
typedef __attribute__((address_space(3))) s16x4 lds_s16x4;

constexpr int NB = 8, SQ = 128, DM = 576, NH = 32, SK = 1024, DH = 128, HD = NH * DH;
constexpr int TILE_BYTES = 16384;
constexpr int PAIR_BYTES = 2 * TILE_BYTES;
constexpr int NP = 4;
constexpr int NPA = DM / 64;
constexpr int NPB = SK / 64;
constexpr int NPAIR = NPA + NPB;
constexpr int K1_LDS = NP * PAIR_BYTES;
constexpr float SCALE_L2E = 0.088388347648318447f * 1.4426950408889634f;

#define DI __device__ __forceinline__
DI unsigned cvtpk(float lo, float hi) { f32x2 v = {lo, hi}; bf16x2 b = __builtin_convertvector(v, bf16x2); return __builtin_bit_cast(unsigned, b); }
DI s16x4 tr_read(const char* p) { return __builtin_amdgcn_ds_read_tr16_b64_v4i16((lds_s16x4*)(p)); }
DI void glds_asm(const void* gsrc, const char* lds_dst) {
  unsigned keep; const unsigned d = __builtin_amdgcn_readfirstlane((unsigned)(size_t)lds_dst);
  asm volatile("s_mov_b32 %0, m0\n\ts_mov_b32 m0, %2\n\ts_nop 0\n\tglobal_load_lds_dwordx4 %1, off\n\ts_mov_b32 m0, %0" : "=&s"(keep) : "v"(gsrc), "s"(d) : "memory");
}
DI void glds_asm_nt(const void* gsrc, const char* lds_dst) {
  unsigned keep; const unsigned d = __builtin_amdgcn_readfirstlane((unsigned)(size_t)lds_dst);
  asm volatile("s_mov_b32 %0, m0\n\ts_mov_b32 m0, %2\n\ts_nop 0\n\tglobal_load_lds_dwordx4 %1, off nt\n\ts_mov_b32 m0, %0" : "=&s"(keep) : "v"(gsrc), "s"(d) : "memory");
}
#define GLDS(src, dst, aux) do { if ((aux) == 2) glds_asm_nt((src), (dst)); else glds_asm((src), (dst)); } while (0)

template <int N> DI void wait_vm() { asm volatile("s_waitcnt vmcnt(%0)" ::"n"(N) : "memory"); }

DI bf16x8 pack_nat(const f32x16& a, int base) {
  unsigned a0 = cvtpk(a[base + 0], a[base + 1]), a1 = cvtpk(a[base + 2], a[base + 3]);
  unsigned b0 = cvtpk(a[base + 4], a[base + 5]), b1 = cvtpk(a[base + 6], a[base + 7]);
  auto r0 = __builtin_amdgcn_permlane32_swap(a0, b0, false, false);
  auto r1 = __builtin_amdgcn_permlane32_swap(a1, b1, false, false);
  u32x4 w = {r0[0], r1[0], r0[1], r1[1]};
  return __builtin_bit_cast(bf16x8, w);
}
DI bf16x8 pack_raw(const f32x16& a, int base) {
  u32x4 w = {cvtpk(a[base + 0], a[base + 1]), cvtpk(a[base + 2], a[base + 3]), cvtpk(a[base + 4], a[base + 5]), cvtpk(a[base + 6], a[base + 7])};
  return __builtin_bit_cast(bf16x8, w);
}

__global__ __launch_bounds__(256, 1)
void attn_fused_kernel(const __hip_bfloat16* __restrict__ x, const __hip_bfloat16* __restrict__ kc, const __hip_bfloat16* __restrict__ vc,
                       const __hip_bfloat16* __restrict__ wq, __hip_bfloat16* __restrict__ obuf) {
  extern __shared__ __attribute__((aligned(16))) char shm[];
  const int tid = threadIdx.x, lane = tid & 63, r32 = lane & 31, hi = lane >> 5;
  const int w = __builtin_amdgcn_readfirstlane(tid >> 6);
  const int bh = blockIdx.x, b = bh >> 5, h = bh & 31;

  const int l4 = lane >> 4;
  const char* kbase = (const char*)kc + (size_t)bh * SK * DH * 2;
  const char* vbase = (const char*)vc + (size_t)bh * SK * DH * 2;
  const char* wbase = (const char*)wq + (size_t)h * DH * 2;
  const char* xbase = (const char*)x + (size_t)b * SQ * DM * 2;
  auto issue_pair = [&](int pi) {
    char* slot = shm + (pi % NP) * PAIR_BYTES;
    if (pi < NPA) {
#pragma unroll
      for (int ii = 0; ii < 4; ++ii) {
        const int row = w * 16 + ii * 4 + l4;
        const int ch = (lane & 15) ^ ((l4 << 2) | ii);
        GLDS(wbase + (size_t)(pi * 64 + row) * (HD * 2) + ch * 16, slot + (w * 4 + ii) * 1024, 0);
      }
#pragma unroll
      for (int ii = 0; ii < 4; ++ii) {
        const int row = (w * 4 + ii) * 8 + (lane >> 3);
        const int ch = (lane & 7) ^ ((ii & 1) * 4 + l4);
        GLDS(xbase + (size_t)row * (DM * 2) + pi * 128 + ch * 16, slot + TILE_BYTES + (w * 4 + ii) * 1024, 0);
      }
    } else {
      const int t = pi - NPA;
#pragma unroll
      for (int ii = 0; ii < 4; ++ii) {
        const int row = w * 16 + ii * 4 + l4;
        const int ch = (lane & 15) ^ ((l4 << 2) | ii);
        GLDS(kbase + (size_t)t * TILE_BYTES + row * 256 + ch * 16, slot + (w * 4 + ii) * 1024, 2);
      }
#pragma unroll
      for (int ii = 0; ii < 4; ++ii) {
        const int row = w * 16 + ii * 4 + l4;
        const int ch = (lane & 15) ^ ((l4 << 2) | ii);
        GLDS(vbase + (size_t)t * TILE_BYTES + row * 256 + ch * 16, slot + TILE_BYTES + (w * 4 + ii) * 1024, 2);
      }
    }
  };
  auto wait_pair = [&](int pi) {
    const int rem = NPAIR - 1 - pi;
    if (rem >= NP - 2) wait_vm<8 * (NP - 2)>();
    else if (rem == 1) wait_vm<8>();
    else wait_vm<0>();
    asm volatile("s_waitcnt lgkmcnt(0)" ::: "memory");
    __builtin_amdgcn_s_barrier();
    if (pi + NP - 1 < NPAIR) issue_pair(pi + NP - 1);
  };

#pragma unroll
  for (int pi = 0; pi < NP - 1; ++pi) issue_pair(pi);

  const int q4 = (lane & 15) >> 2, p4 = lane & 3, blk = (lane >> 4) & 1;
  int trn[2][4], trp[2][4];
#pragma unroll
  for (int t = 0; t < 2; ++t)
#pragma unroll
    for (int c = 0; c < 4; ++c) {
      const int ch = 4 * c + 2 * blk + (p4 >> 1);
      { const int row = 8 * hi + 4 * t + q4; const int sw = ((row & 3) << 2) | ((row >> 2) & 3); trn[t][c] = 256 * row + 16 * (ch ^ sw) + 8 * (p4 & 1); }
      { const int row = 8 * t + 4 * hi + q4; const int sw = ((row & 3) << 2) | ((row >> 2) & 3); trp[t][c] = 256 * row + 16 * (ch ^ sw) + 8 * (p4 & 1); }
    }
  const int swr = ((r32 & 3) << 2) | ((r32 >> 2) & 3);
  const int krow = 256 * r32 + 16 * (hi ^ swr);
  const int xq = 32 * w + r32;
  const int xrow = 128 * xq + 16 * (hi ^ ((xq >> 1) & 7));

  f32x16 acc[4];
#pragma unroll
  for (int c = 0; c < 4; ++c) acc[c] = f32x16{};
  for (int pi = 0; pi < NPA; ++pi) {
    wait_pair(pi);
    const char* slot = shm + (pi % NP) * PAIR_BYTES;
#pragma unroll
    for (int ks = 0; ks < 4; ++ks) {
      const bf16x8 bx = *(const bf16x8*)(slot + TILE_BYTES + (xrow ^ (32 * ks)));
#pragma unroll
      for (int c = 0; c < 4; ++c) {
        const s16x4 lo = tr_read(slot + trn[0][c] + 4096 * ks), hi4 = tr_read(slot + trn[1][c] + 4096 * ks);
        const bf16x8 aw = __builtin_shufflevector(lo, hi4, 0, 1, 2, 3, 4, 5, 6, 7);
        acc[c] = __builtin_amdgcn_mfma_f32_32x32x16_bf16(aw, bx, acc[c], 0, 0, 0);
      }
    }
  }
  bf16x8 qf[8];
#pragma unroll
  for (int c = 0; c < 4; ++c) { qf[2 * c] = pack_nat(acc[c], 0); qf[2 * c + 1] = pack_nat(acc[c], 8); }

  f32x16 o[4];
#pragma unroll
  for (int c = 0; c < 4; ++c) o[c] = f32x16{};
  float m_run = -__builtin_inff(), l_run = 0.f;
  for (int pi = NPA; pi < NPAIR; ++pi) {
    wait_pair(pi);
    const char* slot = shm + (pi % NP) * PAIR_BYTES;
    f32x16 p0 = f32x16{}, p1 = f32x16{};
#pragma unroll
    for (int s = 0; s < 8; ++s) {
      const bf16x8 a0 = *(const bf16x8*)(slot + (krow ^ (32 * s)));
      const bf16x8 a1 = *(const bf16x8*)(slot + 8192 + (krow ^ (32 * s)));
      p0 = __builtin_amdgcn_mfma_f32_32x32x16_bf16(a0, qf[s], p0, 0, 0, 0);
      p1 = __builtin_amdgcn_mfma_f32_32x32x16_bf16(a1, qf[s], p1, 0, 0, 0);
    }
    float mx = p0[0];
#pragma unroll
    for (int r = 1; r < 16; ++r) mx = fmaxf(mx, p0[r]);
#pragma unroll
    for (int r = 0; r < 16; ++r) mx = fmaxf(mx, p1[r]);
    { auto rr = __builtin_amdgcn_permlane32_swap(__float_as_uint(mx), __float_as_uint(mx), false, false);
      mx = fmaxf(__uint_as_float(rr[0]), __uint_as_float(rr[1])); }
    const float m_new = fmaxf(m_run, mx);
    if (__any(m_new > m_run)) {
      const float alpha = __builtin_amdgcn_exp2f((m_run - m_new) * SCALE_L2E);
#pragma unroll
      for (int c = 0; c < 4; ++c)
#pragma unroll
        for (int r = 0; r < 16; ++r) o[c][r] *= alpha;
      l_run *= alpha;
      m_run = m_new;
    }
    const float nm = -m_run * SCALE_L2E;
    float ps = 0.f;
#pragma unroll
    for (int r = 0; r < 16; ++r) { p0[r] = __builtin_amdgcn_exp2f(fmaf(p0[r], SCALE_L2E, nm)); ps += p0[r]; }
#pragma unroll
    for (int r = 0; r < 16; ++r) { p1[r] = __builtin_amdgcn_exp2f(fmaf(p1[r], SCALE_L2E, nm)); ps += p1[r]; }
    l_run += ps;
    bf16x8 pb[4];
    pb[0] = pack_raw(p0, 0); pb[1] = pack_raw(p0, 8); pb[2] = pack_raw(p1, 0); pb[3] = pack_raw(p1, 8);
    const char* vs = slot + TILE_BYTES;
#pragma unroll
    for (int c = 0; c < 4; ++c)
#pragma unroll
      for (int ks = 0; ks < 4; ++ks) {
        const s16x4 lo = tr_read(vs + trp[0][c] + 4096 * ks), hi4 = tr_read(vs + trp[1][c] + 4096 * ks);
        const bf16x8 av = __builtin_shufflevector(lo, hi4, 0, 1, 2, 3, 4, 5, 6, 7);
        o[c] = __builtin_amdgcn_mfma_f32_32x32x16_bf16(av, pb[ks], o[c], 0, 0, 0);
      }
  }
  { auto rr = __builtin_amdgcn_permlane32_swap(__float_as_uint(l_run), __float_as_uint(l_run), false, false);
    l_run = __uint_as_float(rr[0]) + __uint_as_float(rr[1]); }
  const float inv = 1.f / l_run;
  char* orow = (char*)obuf + ((size_t)(b * SQ + 32 * w + r32) * HD + h * DH) * 2;
#pragma unroll
  for (int c = 0; c < 4; ++c)
#pragma unroll
    for (int g = 0; g < 4; ++g) {
      u32x2 v = {cvtpk(o[c][4 * g] * inv, o[c][4 * g + 1] * inv), cvtpk(o[c][4 * g + 2] * inv, o[c][4 * g + 3] * inv)};
      *(u32x2*)(orow + (32 * c + 8 * g + 4 * hi) * 2) = v;
    }
}

constexpr int K2_KT = 64;
__global__ __launch_bounds__(256, 1)
void outproj_kernel(const __hip_bfloat16* __restrict__ obuf, const __hip_bfloat16* __restrict__ wo, __hip_bfloat16* __restrict__ out) {
  extern __shared__ __attribute__((aligned(16))) char shm[];
  const int tid = threadIdx.x, lane = tid & 63, r32 = lane & 31, hi = lane >> 5;
  const int w = __builtin_amdgcn_readfirstlane(tid >> 6);
  const int wm = w & 1, wn = w >> 1;
  const int m0 = blockIdx.x * 64, n0 = blockIdx.y * 64;
  const int l3 = lane >> 3;
  auto issue = [&](int kt, int buf) {
    char* slot = shm + buf * 16384;
#pragma unroll
    for (int ii = 0; ii < 2; ++ii) {
      const int row = (w * 2 + ii) * 8 + l3;
      const int ch = (lane & 7) ^ ((row >> 1) & 7);
      GLDS((const char*)wo + ((size_t)(kt * 64 + row) * DM + n0) * 2 + ch * 16, slot + (w * 2 + ii) * 1024, 0);
    }
#pragma unroll
    for (int ii = 0; ii < 2; ++ii) {
      const int row = (w * 2 + ii) * 8 + l3;
      const int ch = (lane & 7) ^ ((row >> 1) & 7);
      GLDS((const char*)obuf + ((size_t)(m0 + row) * HD + kt * 64) * 2 + ch * 16, slot + 8192 + (w * 2 + ii) * 1024, 0);
    }
  };
  const int q4 = (lane & 15) >> 2, p4 = lane & 3, blk = (lane >> 4) & 1;
  int tra[2];
#pragma unroll
  for (int t = 0; t < 2; ++t) {
    const int row = 8 * hi + 4 * t + q4;
    const int ch = 4 * wn + 2 * blk + (p4 >> 1);
    tra[t] = 128 * row + 16 * (ch ^ ((row >> 1) & 7)) + 8 * (p4 & 1);
  }
  const int mrow = 32 * wm + r32;
  const int orow = 128 * mrow + 16 * (hi ^ ((mrow >> 1) & 7));
  f32x16 acc = f32x16{};
  constexpr int NKT = HD / K2_KT;
  issue(0, 0);
  for (int kt = 0; kt < NKT; ++kt) {
    const int buf = kt & 1;
    wait_vm<0>();
    asm volatile("s_waitcnt lgkmcnt(0)" ::: "memory");
    __builtin_amdgcn_s_barrier();
    if (kt + 1 < NKT) issue(kt + 1, buf ^ 1);
    const char* slot = shm + buf * 16384;
#pragma unroll
    for (int ks = 0; ks < 4; ++ks) {
      const bf16x8 bo = *(const bf16x8*)(slot + 8192 + (orow ^ (32 * ks)));
      const s16x4 lo = tr_read(slot + tra[0] + 2048 * ks), hi4 = tr_read(slot + tra[1] + 2048 * ks);
      const bf16x8 aw = __builtin_shufflevector(lo, hi4, 0, 1, 2, 3, 4, 5, 6, 7);
      acc = __builtin_amdgcn_mfma_f32_32x32x16_bf16(aw, bo, acc, 0, 0, 0);
    }
  }
  char* dst = (char*)out + ((size_t)(m0 + mrow) * DM + n0 + 32 * wn) * 2;
#pragma unroll
  for (int g = 0; g < 4; ++g) {
    u32x2 v = {cvtpk(acc[4 * g], acc[4 * g + 1]), cvtpk(acc[4 * g + 2], acc[4 * g + 3])};
    *(u32x2*)(dst + (8 * g + 4 * hi) * 2) = v;
  }
}

extern "C" void kernel_launch(void* const* d_in, const int* in_sizes, int n_in,
                              void* d_out, int out_size, void* d_ws, size_t ws_size,
                              hipStream_t stream) {
  const __hip_bfloat16* x  = (const __hip_bfloat16*)d_in[0];
  const __hip_bfloat16* kc = (const __hip_bfloat16*)d_in[1];
  const __hip_bfloat16* vc = (const __hip_bfloat16*)d_in[2];
  const __hip_bfloat16* wq = (const __hip_bfloat16*)d_in[3];
  const __hip_bfloat16* wo = (const __hip_bfloat16*)d_in[4];
  __hip_bfloat16* obuf = (__hip_bfloat16*)d_ws;
  __hip_bfloat16* out = (__hip_bfloat16*)d_out;
  hipFuncSetAttribute((const void*)attn_fused_kernel, hipFuncAttributeMaxDynamicSharedMemorySize, K1_LDS);
  hipLaunchKernelGGL(attn_fused_kernel, dim3(NB * NH), dim3(256), K1_LDS, stream, x, kc, vc, wq, obuf);
  hipLaunchKernelGGL(outproj_kernel, dim3(16, 9), dim3(256), 32768, stream, (const __hip_bfloat16*)obuf, wo, out);
}
```

```cpp
#include <hip/hip_runtime.h>
#include <hip/hip_bf16.h>

typedef short    bf16x8 __attribute__((ext_vector_type(8)));
typedef short    s16x4  __attribute__((ext_vector_type(4)));
typedef float    f32x16 __attribute__((ext_vector_type(16)));
typedef float    f32x2  __attribute__((ext_vector_type(2)));
typedef __bf16   bf16x2 __attribute__((ext_vector_type(2)));
typedef unsigned u32x4  __attribute__((ext_vector_type(4)));
typedef unsigned u32x2  __attribute__((ext_vector_type(2)));
typedef __attribute__((address_space(3))) s16x4 lds_s16x4;

constexpr int NB = 8, SQ = 128, DM = 576, NH = 32, SK = 1024, DH = 128, HD = NH * DH;
constexpr int TILE_BYTES = 16384;
constexpr int PAIR_BYTES = 2 * TILE_BYTES;
constexpr int NP = 4;
constexpr int NPA = DM / 64;
constexpr int NPB = SK / 64;
constexpr int NPAIR = NPA + NPB;
constexpr int K1_LDS = NP * PAIR_BYTES;
constexpr float SCALE_L2E = 0.088388347648318447f * 1.4426950408889634f;

#define DI __device__ __forceinline__
DI unsigned cvtpk(float lo, float hi) { f32x2 v = {lo, hi}; bf16x2 b = __builtin_convertvector(v, bf16x2); return __builtin_bit_cast(unsigned, b); }
DI s16x4 tr_read(const char* p) { return __builtin_amdgcn_ds_read_tr16_b64_v4i16((lds_s16x4*)(p)); }
DI void glds_asm(const void* gsrc, const char* lds_dst) {
  unsigned keep; const unsigned d = __builtin_amdgcn_readfirstlane((unsigned)(size_t)lds_dst);
  asm volatile("s_mov_b32 %0, m0\n\ts_mov_b32 m0, %2\n\ts_nop 0\n\tglobal_load_lds_dwordx4 %1, off\n\ts_mov_b32 m0, %0" : "=&s"(keep) : "v"(gsrc), "s"(d) : "memory");
}
DI void glds_asm_nt(const void* gsrc, const char* lds_dst) {
  unsigned keep; const unsigned d = __builtin_amdgcn_readfirstlane((unsigned)(size_t)lds_dst);
  asm volatile("s_mov_b32 %0, m0\n\ts_mov_b32 m0, %2\n\ts_nop 0\n\tglobal_load_lds_dwordx4 %1, off nt\n\ts_mov_b32 m0, %0" : "=&s"(keep) : "v"(gsrc), "s"(d) : "memory");
}
#define GLDS(src, dst, aux) do { if ((aux) == 2) glds_asm_nt((src), (dst)); else glds_asm((src), (dst)); } while (0)

template <int N> DI void wait_vm() { asm volatile("s_waitcnt vmcnt(%0)" ::"n"(N) : "memory"); }

DI bf16x8 pack_nat(const f32x16& a, int base) {
  unsigned a0 = cvtpk(a[base + 0], a[base + 1]), a1 = cvtpk(a[base + 2], a[base + 3]);
  unsigned b0 = cvtpk(a[base + 4], a[base + 5]), b1 = cvtpk(a[base + 6], a[base + 7]);
  auto r0 = __builtin_amdgcn_permlane32_swap(a0, b0, false, false);
  auto r1 = __builtin_amdgcn_permlane32_swap(a1, b1, false, false);
  u32x4 w = {r0[0], r1[0], r0[1], r1[1]};
  return __builtin_bit_cast(bf16x8, w);
}
DI bf16x8 pack_raw(const f32x16& a, int base) {
  u32x4 w = {cvtpk(a[base + 0], a[base + 1]), cvtpk(a[base + 2], a[base + 3]), cvtpk(a[base + 4], a[base + 5]), cvtpk(a[base + 6], a[base + 7])};
  return __builtin_bit_cast(bf16x8, w);
}

__global__ __launch_bounds__(512, 2)
void attn_fused_kernel(const __hip_bfloat16* __restrict__ x, const __hip_bfloat16* __restrict__ kc, const __hip_bfloat16* __restrict__ vc,
                       const __hip_bfloat16* __restrict__ wq, __hip_bfloat16* __restrict__ obuf) {
  extern __shared__ __attribute__((aligned(16))) char shm[];
  const int tid = threadIdx.x, lane = tid & 63, r32 = lane & 31, hi = lane >> 5;
  const int w8 = __builtin_amdgcn_readfirstlane(tid >> 6);
  const int w = w8 & 3, grp = w8 >> 2;
  const int bh = blockIdx.x, b = bh >> 5, h = bh & 31;

  const int l4 = lane >> 4;
  const char* kbase = (const char*)kc + (size_t)bh * SK * DH * 2;
  const char* vbase = (const char*)vc + (size_t)bh * SK * DH * 2;
  const char* wbase = (const char*)wq + (size_t)h * DH * 2;
  const char* xbase = (const char*)x + (size_t)b * SQ * DM * 2;
  auto issue_pair = [&](int pi) {
    char* slot = shm + (pi % NP) * PAIR_BYTES;
#pragma unroll
    for (int ii = 0; ii < 2; ++ii) {
      const int j = w8 * 2 + ii;
      const int row = 4 * j + l4;
      const int ch = (lane & 15) ^ ((l4 << 2) | (j & 3));
      if (pi < NPA) {
        GLDS(wbase + (size_t)(pi * 64 + row) * (HD * 2) + ch * 16, slot + j * 1024, 0);
        const int xr = 8 * j + (lane >> 3);
        const int xc = (lane & 7) ^ ((j & 1) * 4 + l4);
        GLDS(xbase + (size_t)xr * (DM * 2) + pi * 128 + xc * 16, slot + TILE_BYTES + j * 1024, 0);
      } else {
        const int t = pi - NPA;
        GLDS(kbase + (size_t)t * TILE_BYTES + row * 256 + ch * 16, slot + j * 1024, 2);
        GLDS(vbase + (size_t)t * TILE_BYTES + row * 256 + ch * 16, slot + TILE_BYTES + j * 1024, 2);
      }
    }
  };
  int nxt = 0;
  for (; nxt < NP - 1; ++nxt) issue_pair(nxt);

  const int q4 = (lane & 15) >> 2, p4 = lane & 3, blk = (lane >> 4) & 1;
  int trn[2][2], trp[2][4];
#pragma unroll
  for (int t = 0; t < 2; ++t) {
#pragma unroll
    for (int c = 0; c < 4; ++c) {
      const int ch = 4 * c + 2 * blk + (p4 >> 1);
      const int row = 8 * t + 4 * hi + q4; const int sw = ((row & 3) << 2) | ((row >> 2) & 3);
      trp[t][c] = 256 * row + 16 * (ch ^ sw) + 8 * (p4 & 1);
    }
#pragma unroll
    for (int cc = 0; cc < 2; ++cc) {
      const int ch = 4 * (2 * grp + cc) + 2 * blk + (p4 >> 1);
      const int row = 8 * hi + 4 * t + q4; const int sw = ((row & 3) << 2) | ((row >> 2) & 3);
      trn[t][cc] = 256 * row + 16 * (ch ^ sw) + 8 * (p4 & 1);
    }
  }
  const int swr = ((r32 & 3) << 2) | ((r32 >> 2) & 3);
  const int krow = 256 * r32 + 16 * (hi ^ swr);
  const int xq = 32 * w + r32;
  const int xrow = 128 * xq + 16 * (hi ^ ((xq >> 1) & 7));

  f32x16 acc[2] = {f32x16{}, f32x16{}};
  for (int pi = 0; pi < NPA; ++pi) {
    wait_vm<8>();
    asm volatile("s_waitcnt lgkmcnt(0)" ::: "memory");
    __builtin_amdgcn_s_barrier();
    if (nxt < NPAIR) { issue_pair(nxt); ++nxt; }
    const char* slot = shm + (pi % NP) * PAIR_BYTES;
    bf16x8 bx[4], aw[4][2];
#pragma unroll
    for (int ks = 0; ks < 4; ++ks) {
      bx[ks] = *(const bf16x8*)(slot + TILE_BYTES + (xrow ^ (32 * ks)));
#pragma unroll
      for (int cc = 0; cc < 2; ++cc) {
        const s16x4 lo = tr_read(slot + trn[0][cc] + 4096 * ks), hi4 = tr_read(slot + trn[1][cc] + 4096 * ks);
        aw[ks][cc] = __builtin_shufflevector(lo, hi4, 0, 1, 2, 3, 4, 5, 6, 7);
      }
    }
    __builtin_amdgcn_sched_barrier(0);
#pragma unroll
    for (int ks = 0; ks < 4; ++ks)
#pragma unroll
      for (int cc = 0; cc < 2; ++cc) acc[cc] = __builtin_amdgcn_mfma_f32_32x32x16_bf16(aw[ks][cc], bx[ks], acc[cc], 0, 0, 0);
  }
  bf16x8 qf[8];
  {
    char* xch = shm + K1_LDS;
#pragma unroll
    for (int cc = 0; cc < 2; ++cc) {
      qf[4 * grp + 2 * cc] = pack_nat(acc[cc], 0); qf[4 * grp + 2 * cc + 1] = pack_nat(acc[cc], 8);
      *(bf16x8*)(xch + ((w * 8 + 4 * grp + 2 * cc) * 64 + lane) * 16) = qf[4 * grp + 2 * cc];
      *(bf16x8*)(xch + ((w * 8 + 4 * grp + 2 * cc + 1) * 64 + lane) * 16) = qf[4 * grp + 2 * cc + 1];
    }
    asm volatile("s_waitcnt lgkmcnt(0)" ::: "memory");
    __builtin_amdgcn_s_barrier();
#pragma unroll
    for (int s = 0; s < 8; ++s) qf[s] = *(const bf16x8*)(xch + ((w * 8 + s) * 64 + lane) * 16);
  }

  f32x16 o[4];
#pragma unroll
  for (int c = 0; c < 4; ++c) o[c] = f32x16{};
  float m_run = -__builtin_inff(), l_run = 0.f;
  for (int P = NPA; P < NPAIR; P += 2) {
    wait_vm<0>();
    asm volatile("s_waitcnt lgkmcnt(0)" ::: "memory");
    __builtin_amdgcn_s_barrier();
    while (nxt < NPAIR && nxt < P + NP) { issue_pair(nxt); ++nxt; }
    const char* slot = shm + ((P + grp) % NP) * PAIR_BYTES;
    f32x16 p0 = f32x16{}, p1 = f32x16{};
    bf16x8 kf0[8], kf1[8];
#pragma unroll
    for (int s = 0; s < 8; ++s) {
      kf0[s] = *(const bf16x8*)(slot + (krow ^ (32 * s)));
      kf1[s] = *(const bf16x8*)(slot + 8192 + (krow ^ (32 * s)));
    }
    __builtin_amdgcn_sched_barrier(0);
#pragma unroll
    for (int s = 0; s < 8; ++s) {
      p0 = __builtin_amdgcn_mfma_f32_32x32x16_bf16(kf0[s], qf[s], p0, 0, 0, 0);
      p1 = __builtin_amdgcn_mfma_f32_32x32x16_bf16(kf1[s], qf[s], p1, 0, 0, 0);
    }
    __builtin_amdgcn_sched_barrier(0);
    bf16x8 av[4][4];
    { const char* vs = slot + TILE_BYTES;
#pragma unroll
      for (int c = 0; c < 2; ++c)
#pragma unroll
        for (int ks = 0; ks < 4; ++ks) {
          const s16x4 lo = tr_read(vs + trp[0][c] + 4096 * ks), hi4 = tr_read(vs + trp[1][c] + 4096 * ks);
          av[c][ks] = __builtin_shufflevector(lo, hi4, 0, 1, 2, 3, 4, 5, 6, 7);
        } }
    __builtin_amdgcn_sched_barrier(0);
    float mx = p0[0];
#pragma unroll
    for (int r = 1; r < 16; ++r) mx = fmaxf(mx, p0[r]);
#pragma unroll
    for (int r = 0; r < 16; ++r) mx = fmaxf(mx, p1[r]);
    { auto rr = __builtin_amdgcn_permlane32_swap(__float_as_uint(mx), __float_as_uint(mx), false, false);
      mx = fmaxf(__uint_as_float(rr[0]), __uint_as_float(rr[1])); }
    const float m_new = fmaxf(m_run, mx);
    if (__any(m_new > m_run)) {
      const float alpha = __builtin_amdgcn_exp2f((m_run - m_new) * SCALE_L2E);
#pragma unroll
      for (int c = 0; c < 4; ++c)
#pragma unroll
        for (int r = 0; r < 16; ++r) o[c][r] *= alpha;
      l_run *= alpha;
      m_run = m_new;
    }
    const float nm = -m_run * SCALE_L2E;
    float ps = 0.f;
#pragma unroll
    for (int r = 0; r < 16; ++r) { p0[r] = __builtin_amdgcn_exp2f(fmaf(p0[r], SCALE_L2E, nm)); ps += p0[r]; }
#pragma unroll
    for (int r = 0; r < 16; ++r) { p1[r] = __builtin_amdgcn_exp2f(fmaf(p1[r], SCALE_L2E, nm)); ps += p1[r]; }
    l_run += ps;
    bf16x8 pb[4];
    pb[0] = pack_raw(p0, 0); pb[1] = pack_raw(p0, 8); pb[2] = pack_raw(p1, 0); pb[3] = pack_raw(p1, 8);
    __builtin_amdgcn_sched_barrier(0);
    { const char* vs = slot + TILE_BYTES;
#pragma unroll
      for (int c = 2; c < 4; ++c)
#pragma unroll
        for (int ks = 0; ks < 4; ++ks) {
          const s16x4 lo = tr_read(vs + trp[0][c] + 4096 * ks), hi4 = tr_read(vs + trp[1][c] + 4096 * ks);
          av[c][ks] = __builtin_shufflevector(lo, hi4, 0, 1, 2, 3, 4, 5, 6, 7);
        } }
    __builtin_amdgcn_sched_barrier(0);
#pragma unroll
    for (int c = 0; c < 4; ++c)
#pragma unroll
      for (int ks = 0; ks < 4; ++ks) o[c] = __builtin_amdgcn_mfma_f32_32x32x16_bf16(av[c][ks], pb[ks], o[c], 0, 0, 0);
  }
  { auto rr = __builtin_amdgcn_permlane32_swap(__float_as_uint(l_run), __float_as_uint(l_run), false, false);
    l_run = __uint_as_float(rr[0]) + __uint_as_float(rr[1]); }
  asm volatile("s_waitcnt lgkmcnt(0)" ::: "memory");
  __builtin_amdgcn_s_barrier();
  float* mg = (float*)shm + w * (66 * 64);
  if (grp == 1) {
#pragma unroll
    for (int c = 0; c < 4; ++c)
#pragma unroll
      for (int r = 0; r < 16; ++r) mg[(c * 16 + r) * 64 + lane] = o[c][r];
    mg[64 * 64 + lane] = m_run; mg[65 * 64 + lane] = l_run;
  }
  asm volatile("s_waitcnt lgkmcnt(0)" ::: "memory");
  __builtin_amdgcn_s_barrier();
  if (grp == 0) {
    const float m1 = mg[64 * 64 + lane], l1 = mg[65 * 64 + lane];
    const float mm = fmaxf(m_run, m1);
    const float a0 = __builtin_amdgcn_exp2f((m_run - mm) * SCALE_L2E), a1 = __builtin_amdgcn_exp2f((m1 - mm) * SCALE_L2E);
    const float inv = 1.f / (l_run * a0 + l1 * a1);
    const float s0 = a0 * inv, s1 = a1 * inv;
    char* orow = (char*)obuf + ((size_t)(b * SQ + 32 * w + r32) * HD + h * DH) * 2;
#pragma unroll
    for (int c = 0; c < 4; ++c)
#pragma unroll
      for (int g = 0; g < 4; ++g) {
        float v[4];
#pragma unroll
        for (int e = 0; e < 4; ++e) v[e] = o[c][4 * g + e] * s0 + mg[(c * 16 + 4 * g + e) * 64 + lane] * s1;
        u32x2 pk = {cvtpk(v[0], v[1]), cvtpk(v[2], v[3])};
        *(u32x2*)(orow + (32 * c + 8 * g + 4 * hi) * 2) = pk;
      }
  }
}

constexpr int K2_KT = 64, K2_NS = 8;
__global__ __launch_bounds__(256, 1)
void outproj_kernel(const __hip_bfloat16* __restrict__ obuf, const __hip_bfloat16* __restrict__ wo, __hip_bfloat16* __restrict__ out) {
  extern __shared__ __attribute__((aligned(16))) char shm[];
  const int tid = threadIdx.x, lane = tid & 63, r32 = lane & 31, hi = lane >> 5;
  const int w = __builtin_amdgcn_readfirstlane(tid >> 6);
  const int wm = w & 1, wn = w >> 1;
  const int m0 = blockIdx.x * 64, n0 = blockIdx.y * 64;
  const int l3 = lane >> 3;
  auto issue = [&](int kt, int buf) {
    char* slot = shm + buf * 16384;
#pragma unroll
    for (int ii = 0; ii < 2; ++ii) {
      const int row = (w * 2 + ii) * 8 + l3;
      const int ch = (lane & 7) ^ ((row >> 1) & 7);
      GLDS((const char*)wo + ((size_t)(kt * 64 + row) * DM + n0) * 2 + ch * 16, slot + (w * 2 + ii) * 1024, 0);
    }
#pragma unroll
    for (int ii = 0; ii < 2; ++ii) {
      const int row = (w * 2 + ii) * 8 + l3;
      const int ch = (lane & 7) ^ ((row >> 1) & 7);
      GLDS((const char*)obuf + ((size_t)(m0 + row) * HD + kt * 64) * 2 + ch * 16, slot + 8192 + (w * 2 + ii) * 1024, 0);
    }
  };
  const int q4 = (lane & 15) >> 2, p4 = lane & 3, blk = (lane >> 4) & 1;
  int tra[2];
#pragma unroll
  for (int t = 0; t < 2; ++t) {
    const int row = 8 * hi + 4 * t + q4;
    const int ch = 4 * wn + 2 * blk + (p4 >> 1);
    tra[t] = 128 * row + 16 * (ch ^ ((row >> 1) & 7)) + 8 * (p4 & 1);
  }
  const int mrow = 32 * wm + r32;
  const int orow = 128 * mrow + 16 * (hi ^ ((mrow >> 1) & 7));
  f32x16 acc = f32x16{};
  constexpr int NKT = HD / K2_KT;
#pragma unroll
  for (int i = 0; i < K2_NS - 1; ++i) issue(i, i);
  for (int kt = 0; kt < NKT; ++kt) {
    const int buf = kt % K2_NS;
    if (kt + K2_NS - 2 < NKT) wait_vm<4 * (K2_NS - 2)>(); else wait_vm<0>();
    asm volatile("s_waitcnt lgkmcnt(0)" ::: "memory");
    __builtin_amdgcn_s_barrier();
    if (kt + K2_NS - 1 < NKT) issue(kt + K2_NS - 1, (kt + K2_NS - 1) % K2_NS);
    const char* slot = shm + buf * 16384;
    bf16x8 bo[4], aw[4];
#pragma unroll
    for (int ks = 0; ks < 4; ++ks) {
      bo[ks] = *(const bf16x8*)(slot + 8192 + (orow ^ (32 * ks)));
      const s16x4 lo = tr_read(slot + tra[0] + 2048 * ks), hi4 = tr_read(slot + tra[1] + 2048 * ks);
      aw[ks] = __builtin_shufflevector(lo, hi4, 0, 1, 2, 3, 4, 5, 6, 7);
    }
    __builtin_amdgcn_sched_barrier(0);
#pragma unroll
    for (int ks = 0; ks < 4; ++ks) acc = __builtin_amdgcn_mfma_f32_32x32x16_bf16(aw[ks], bo[ks], acc, 0, 0, 0);
  }
  char* dst = (char*)out + ((size_t)(m0 + mrow) * DM + n0 + 32 * wn) * 2;
#pragma unroll
  for (int g = 0; g < 4; ++g) {
    u32x2 v = {cvtpk(acc[4 * g], acc[4 * g + 1]), cvtpk(acc[4 * g + 2], acc[4 * g + 3])};
    *(u32x2*)(dst + (8 * g + 4 * hi) * 2) = v;
  }
}

extern "C" void kernel_launch(void* const* d_in, const int* in_sizes, int n_in,
                              void* d_out, int out_size, void* d_ws, size_t ws_size,
                              hipStream_t stream) {
  const __hip_bfloat16* x  = (const __hip_bfloat16*)d_in[0];
  const __hip_bfloat16* kc = (const __hip_bfloat16*)d_in[1];
  const __hip_bfloat16* vc = (const __hip_bfloat16*)d_in[2];
  const __hip_bfloat16* wq = (const __hip_bfloat16*)d_in[3];
  const __hip_bfloat16* wo = (const __hip_bfloat16*)d_in[4];
  __hip_bfloat16* obuf = (__hip_bfloat16*)d_ws;
  __hip_bfloat16* out = (__hip_bfloat16*)d_out;
  hipFuncSetAttribute((const void*)attn_fused_kernel, hipFuncAttributeMaxDynamicSharedMemorySize, K1_LDS + 32768);
  hipLaunchKernelGGL(attn_fused_kernel, dim3(NB * NH), dim3(512), K1_LDS + 32768, stream, x, kc, vc, wq, obuf);
  hipFuncSetAttribute((const void*)outproj_kernel, hipFuncAttributeMaxDynamicSharedMemorySize, K2_NS * 16384);
  hipLaunchKernelGGL(outproj_kernel, dim3(16, 9), dim3(256), K2_NS * 16384, stream, (const __hip_bfloat16*)obuf, wo, out);
}
```

```cpp
#include <hip/hip_runtime.h>
#include <hip/hip_bf16.h>

typedef short    bf16x8 __attribute__((ext_vector_type(8)));
typedef short    s16x4  __attribute__((ext_vector_type(4)));
typedef float    f32x16 __attribute__((ext_vector_type(16)));
typedef float    f32x2  __attribute__((ext_vector_type(2)));
typedef __bf16   bf16x2 __attribute__((ext_vector_type(2)));
typedef unsigned u32x4  __attribute__((ext_vector_type(4)));
typedef unsigned u32x2  __attribute__((ext_vector_type(2)));
typedef __attribute__((address_space(3))) s16x4 lds_s16x4;

constexpr int NB = 8, SQ = 128, DM = 576, NH = 32, SK = 1024, DH = 128, HD = NH * DH;
constexpr int TILE_BYTES = 16384;
constexpr int PAIR_BYTES = 2 * TILE_BYTES;
constexpr int NP = 4;
constexpr int NPA = DM / 64;
constexpr int NPB = SK / 64;
constexpr int NPAIR = NPA + NPB;
constexpr int K1_LDS = NP * PAIR_BYTES;
constexpr float SCALE_L2E = 0.088388347648318447f * 1.4426950408889634f;

#define DI __device__ __forceinline__
DI unsigned cvtpk(float lo, float hi) { f32x2 v = {lo, hi}; bf16x2 b = __builtin_convertvector(v, bf16x2); return __builtin_bit_cast(unsigned, b); }
DI s16x4 tr_read(const char* p) { return __builtin_amdgcn_ds_read_tr16_b64_v4i16((lds_s16x4*)(p)); }
DI void glds_asm(const void* gsrc, const char* lds_dst) {
  unsigned keep; const unsigned d = __builtin_amdgcn_readfirstlane((unsigned)(size_t)lds_dst);
  asm volatile("s_mov_b32 %0, m0\n\ts_mov_b32 m0, %2\n\ts_nop 0\n\tglobal_load_lds_dwordx4 %1, off\n\ts_mov_b32 m0, %0" : "=&s"(keep) : "v"(gsrc), "s"(d) : "memory");
}
DI void glds_asm_nt(const void* gsrc, const char* lds_dst) {
  unsigned keep; const unsigned d = __builtin_amdgcn_readfirstlane((unsigned)(size_t)lds_dst);
  asm volatile("s_mov_b32 %0, m0\n\ts_mov_b32 m0, %2\n\ts_nop 0\n\tglobal_load_lds_dwordx4 %1, off nt\n\ts_mov_b32 m0, %0" : "=&s"(keep) : "v"(gsrc), "s"(d) : "memory");
}
#define GLDS(src, dst, aux) do { if ((aux) == 2) glds_asm_nt((src), (dst)); else glds_asm((src), (dst)); } while (0)

template <int N> DI void wait_vm() { asm volatile("s_waitcnt vmcnt(%0)" ::"n"(N) : "memory"); }

DI bf16x8 pack_nat(const f32x16& a, int base) {
  unsigned a0 = cvtpk(a[base + 0], a[base + 1]), a1 = cvtpk(a[base + 2], a[base + 3]);
  unsigned b0 = cvtpk(a[base + 4], a[base + 5]), b1 = cvtpk(a[base + 6], a[base + 7]);
  auto r0 = __builtin_amdgcn_permlane32_swap(a0, b0, false, false);
  auto r1 = __builtin_amdgcn_permlane32_swap(a1, b1, false, false);
  u32x4 w = {r0[0], r1[0], r0[1], r1[1]};
  return __builtin_bit_cast(bf16x8, w);
}
DI bf16x8 pack_raw(const f32x16& a, int base) {
  u32x4 w = {cvtpk(a[base + 0], a[base + 1]), cvtpk(a[base + 2], a[base + 3]), cvtpk(a[base + 4], a[base + 5]), cvtpk(a[base + 6], a[base + 7])};
  return __builtin_bit_cast(bf16x8, w);
}

__global__ __launch_bounds__(512, 2)
void attn_fused_kernel(const __hip_bfloat16* __restrict__ x, const __hip_bfloat16* __restrict__ kc, const __hip_bfloat16* __restrict__ vc,
                       const __hip_bfloat16* __restrict__ wq, __hip_bfloat16* __restrict__ obuf) {
  extern __shared__ __attribute__((aligned(16))) char shm[];
  const int tid = threadIdx.x, lane = tid & 63, r32 = lane & 31, hi = lane >> 5;
  const int w8 = __builtin_amdgcn_readfirstlane(tid >> 6);
  const int w = w8 & 3, grp = w8 >> 2;
  const int bh = blockIdx.x, b = bh >> 5, h = bh & 31;

  const int l4 = lane >> 4;
  const char* kbase = (const char*)kc + (size_t)bh * SK * DH * 2;
  const char* vbase = (const char*)vc + (size_t)bh * SK * DH * 2;
  const char* wbase = (const char*)wq + (size_t)h * DH * 2;
  const char* xbase = (const char*)x + (size_t)b * SQ * DM * 2;
  auto issue_pair = [&](int pi) {
    char* slot = shm + (pi % NP) * PAIR_BYTES;
#pragma unroll
    for (int ii = 0; ii < 2; ++ii) {
      const int j = w8 * 2 + ii;
      const int row = 4 * j + l4;
      const int ch = (lane & 15) ^ ((l4 << 2) | (j & 3));
      if (pi < NPA) {
        GLDS(wbase + (size_t)(pi * 64 + row) * (HD * 2) + ch * 16, slot + j * 1024, 0);
        const int xr = 8 * j + (lane >> 3);
        const int xc = (lane & 7) ^ ((j & 1) * 4 + l4);
        GLDS(xbase + (size_t)xr * (DM * 2) + pi * 128 + xc * 16, slot + TILE_BYTES + j * 1024, 0);
      } else {
        const int t = pi - NPA;
        GLDS(kbase + (size_t)t * TILE_BYTES + row * 256 + ch * 16, slot + j * 1024, 2);
        GLDS(vbase + (size_t)t * TILE_BYTES + row * 256 + ch * 16, slot + TILE_BYTES + j * 1024, 2);
      }
    }
  };
  int nxt = 0;
  for (; nxt < NP - 1; ++nxt) issue_pair(nxt);

  const int q4 = (lane & 15) >> 2, p4 = lane & 3, blk = (lane >> 4) & 1;
  int trn[2][2], trp[2][4];
#pragma unroll
  for (int t = 0; t < 2; ++t) {
#pragma unroll
    for (int c = 0; c < 4; ++c) {
      const int ch = 4 * c + 2 * blk + (p4 >> 1);
      const int row = 8 * t + 4 * hi + q4; const int sw = ((row & 3) << 2) | ((row >> 2) & 3);
      trp[t][c] = 256 * row + 16 * (ch ^ sw) + 8 * (p4 & 1);
    }
#pragma unroll
    for (int cc = 0; cc < 2; ++cc) {
      const int ch = 4 * (2 * grp + cc) + 2 * blk + (p4 >> 1);
      const int row = 8 * hi + 4 * t + q4; const int sw = ((row & 3) << 2) | ((row >> 2) & 3);
      trn[t][cc] = 256 * row + 16 * (ch ^ sw) + 8 * (p4 & 1);
    }
  }
  const int swr = ((r32 & 3) << 2) | ((r32 >> 2) & 3);
  const int krow = 256 * r32 + 16 * (hi ^ swr);
  const int xq = 32 * w + r32;
  const int xrow = 128 * xq + 16 * (hi ^ ((xq >> 1) & 7));

  f32x16 acc[2] = {f32x16{}, f32x16{}};
  for (int pi = 0; pi < NPA; ++pi) {
    wait_vm<8>();
    asm volatile("s_waitcnt lgkmcnt(0)" ::: "memory");
    __builtin_amdgcn_s_barrier();
    if (nxt < NPAIR) { issue_pair(nxt); ++nxt; }
    const char* slot = shm + (pi % NP) * PAIR_BYTES;
    bf16x8 bx[4], aw[4][2];
#pragma unroll
    for (int ks = 0; ks < 4; ++ks) {
      bx[ks] = *(const bf16x8*)(slot + TILE_BYTES + (xrow ^ (32 * ks)));
#pragma unroll
      for (int cc = 0; cc < 2; ++cc) {
        const s16x4 lo = tr_read(slot + trn[0][cc] + 4096 * ks), hi4 = tr_read(slot + trn[1][cc] + 4096 * ks);
        aw[ks][cc] = __builtin_shufflevector(lo, hi4, 0, 1, 2, 3, 4, 5, 6, 7);
      }
    }
    __builtin_amdgcn_sched_barrier(0);
#pragma unroll
    for (int ks = 0; ks < 4; ++ks)
#pragma unroll
      for (int cc = 0; cc < 2; ++cc) acc[cc] = __builtin_amdgcn_mfma_f32_32x32x16_bf16(aw[ks][cc], bx[ks], acc[cc], 0, 0, 0);
  }
  bf16x8 qf[8];
  {
    char* xch = shm + K1_LDS;
#pragma unroll
    for (int cc = 0; cc < 2; ++cc) {
      qf[4 * grp + 2 * cc] = pack_nat(acc[cc], 0); qf[4 * grp + 2 * cc + 1] = pack_nat(acc[cc], 8);
      *(bf16x8*)(xch + ((w * 8 + 4 * grp + 2 * cc) * 64 + lane) * 16) = qf[4 * grp + 2 * cc];
      *(bf16x8*)(xch + ((w * 8 + 4 * grp + 2 * cc + 1) * 64 + lane) * 16) = qf[4 * grp + 2 * cc + 1];
    }
    asm volatile("s_waitcnt lgkmcnt(0)" ::: "memory");
    __builtin_amdgcn_s_barrier();
#pragma unroll
    for (int s = 0; s < 8; ++s) qf[s] = *(const bf16x8*)(xch + ((w * 8 + s) * 64 + lane) * 16);
  }

  f32x16 o[4];
#pragma unroll
  for (int c = 0; c < 4; ++c) o[c] = f32x16{};
  float m_run = -__builtin_inff(), l_run = 0.f;
  for (int P = NPA; P < NPAIR; P += 2) {
    wait_vm<0>();
    asm volatile("s_waitcnt lgkmcnt(0)" ::: "memory");
    __builtin_amdgcn_s_barrier();
    while (nxt < NPAIR && nxt < P + NP) { issue_pair(nxt); ++nxt; }
    const char* slot = shm + ((P + grp) % NP) * PAIR_BYTES;
    f32x16 p0 = f32x16{}, p1 = f32x16{};
    bf16x8 kf0[8], kf1[8];
#pragma unroll
    for (int s = 0; s < 8; ++s) {
      kf0[s] = *(const bf16x8*)(slot + (krow ^ (32 * s)));
      kf1[s] = *(const bf16x8*)(slot + 8192 + (krow ^ (32 * s)));
    }
    __builtin_amdgcn_sched_barrier(0);
#pragma unroll
    for (int s = 0; s < 8; ++s) {
      p0 = __builtin_amdgcn_mfma_f32_32x32x16_bf16(kf0[s], qf[s], p0, 0, 0, 0);
      p1 = __builtin_amdgcn_mfma_f32_32x32x16_bf16(kf1[s], qf[s], p1, 0, 0, 0);
    }
    __builtin_amdgcn_sched_barrier(0);
    bf16x8 av[4][4];
    { const char* vs = slot + TILE_BYTES;
#pragma unroll
      for (int c = 0; c < 2; ++c)
#pragma unroll
        for (int ks = 0; ks < 4; ++ks) {
          const s16x4 lo = tr_read(vs + trp[0][c] + 4096 * ks), hi4 = tr_read(vs + trp[1][c] + 4096 * ks);
          av[c][ks] = __builtin_shufflevector(lo, hi4, 0, 1, 2, 3, 4, 5, 6, 7);
        } }
    __builtin_amdgcn_sched_barrier(0);
    float mx = p0[0];
#pragma unroll
    for (int r = 1; r < 16; ++r) mx = fmaxf(mx, p0[r]);
#pragma unroll
    for (int r = 0; r < 16; ++r) mx = fmaxf(mx, p1[r]);
    { auto rr = __builtin_amdgcn_permlane32_swap(__float_as_uint(mx), __float_as_uint(mx), false, false);
      mx = fmaxf(__uint_as_float(rr[0]), __uint_as_float(rr[1])); }
    const float m_new = fmaxf(m_run, mx);
    if (__any(m_new > m_run)) {
      const float alpha = __builtin_amdgcn_exp2f((m_run - m_new) * SCALE_L2E);
#pragma unroll
      for (int c = 0; c < 4; ++c)
#pragma unroll
        for (int r = 0; r < 16; ++r) o[c][r] *= alpha;
      l_run *= alpha;
      m_run = m_new;
    }
    const float nm = -m_run * SCALE_L2E;
    float ps = 0.f;
#pragma unroll
    for (int r = 0; r < 16; ++r) { p0[r] = __builtin_amdgcn_exp2f(fmaf(p0[r], SCALE_L2E, nm)); ps += p0[r]; }
#pragma unroll
    for (int r = 0; r < 16; ++r) { p1[r] = __builtin_amdgcn_exp2f(fmaf(p1[r], SCALE_L2E, nm)); ps += p1[r]; }
    l_run += ps;
    bf16x8 pb[4];
    pb[0] = pack_raw(p0, 0); pb[1] = pack_raw(p0, 8); pb[2] = pack_raw(p1, 0); pb[3] = pack_raw(p1, 8);
    __builtin_amdgcn_sched_barrier(0);
    { const char* vs = slot + TILE_BYTES;
#pragma unroll
      for (int c = 2; c < 4; ++c)
#pragma unroll
        for (int ks = 0; ks < 4; ++ks) {
          const s16x4 lo = tr_read(vs + trp[0][c] + 4096 * ks), hi4 = tr_read(vs + trp[1][c] + 4096 * ks);
          av[c][ks] = __builtin_shufflevector(lo, hi4, 0, 1, 2, 3, 4, 5, 6, 7);
        } }
    __builtin_amdgcn_sched_barrier(0);
#pragma unroll
    for (int c = 0; c < 4; ++c)
#pragma unroll
      for (int ks = 0; ks < 4; ++ks) o[c] = __builtin_amdgcn_mfma_f32_32x32x16_bf16(av[c][ks], pb[ks], o[c], 0, 0, 0);
  }
  { auto rr = __builtin_amdgcn_permlane32_swap(__float_as_uint(l_run), __float_as_uint(l_run), false, false);
    l_run = __uint_as_float(rr[0]) + __uint_as_float(rr[1]); }
  asm volatile("s_waitcnt lgkmcnt(0)" ::: "memory");
  __builtin_amdgcn_s_barrier();
  float* mg = (float*)shm + w * (66 * 64);
  if (grp == 1) {
#pragma unroll
    for (int c = 0; c < 4; ++c)
#pragma unroll
      for (int r = 0; r < 16; ++r) mg[(c * 16 + r) * 64 + lane] = o[c][r];
    mg[64 * 64 + lane] = m_run; mg[65 * 64 + lane] = l_run;
  }
  asm volatile("s_waitcnt lgkmcnt(0)" ::: "memory");
  __builtin_amdgcn_s_barrier();
  if (grp == 0) {
    const float m1 = mg[64 * 64 + lane], l1 = mg[65 * 64 + lane];
    const float mm = fmaxf(m_run, m1);
    const float a0 = __builtin_amdgcn_exp2f((m_run - mm) * SCALE_L2E), a1 = __builtin_amdgcn_exp2f((m1 - mm) * SCALE_L2E);
    const float inv = 1.f / (l_run * a0 + l1 * a1);
    const float s0 = a0 * inv, s1 = a1 * inv;
    char* orow = (char*)obuf + ((size_t)bh * SQ + 32 * w + r32) * (DH * 2);
#pragma unroll
    for (int c = 0; c < 4; ++c)
#pragma unroll
      for (int g = 0; g < 4; ++g) {
        float v[4];
#pragma unroll
        for (int e = 0; e < 4; ++e) v[e] = o[c][4 * g + e] * s0 + mg[(c * 16 + 4 * g + e) * 64 + lane] * s1;
        u32x2 pk = {cvtpk(v[0], v[1]), cvtpk(v[2], v[3])};
        *(u32x2*)(orow + (32 * c + 8 * g + 4 * hi) * 2) = pk;
      }
  }
}

constexpr int K2_KT = 64, K2_NS = 4, K2_SPLIT = 8;
__global__ __launch_bounds__(256, 1)
void outproj_kernel(const __hip_bfloat16* __restrict__ obuf, const __hip_bfloat16* __restrict__ wo, float* __restrict__ part) {
  extern __shared__ __attribute__((aligned(16))) char shm[];
  const int tid = threadIdx.x, lane = tid & 63, r32 = lane & 31, hi = lane >> 5;
  const int w = __builtin_amdgcn_readfirstlane(tid >> 6);
  const int wm = w & 1, wn = w >> 1;
  const int bz = blockIdx.x & 7, tile = blockIdx.x >> 3;
  const int m0 = (tile & 15) * 64, n0 = (tile >> 4) * 64;
  const int l3 = lane >> 3;
  auto issue = [&](int kt, int buf) {
    kt += bz * (HD / K2_KT / K2_SPLIT);
    char* slot = shm + buf * 16384;
#pragma unroll
    for (int ii = 0; ii < 2; ++ii) {
      const int row = (w * 2 + ii) * 8 + l3;
      const int ch = (lane & 7) ^ ((row >> 1) & 7);
      GLDS((const char*)wo + ((size_t)(kt * 64 + row) * DM + n0) * 2 + ch * 16, slot + (w * 2 + ii) * 1024, 0);
    }
#pragma unroll
    for (int ii = 0; ii < 2; ++ii) {
      const int row = (w * 2 + ii) * 8 + l3;
      const int ch = (lane & 7) ^ ((row >> 1) & 7);
      GLDS((const char*)obuf + ((size_t)((m0 >> 7) * NH + (kt >> 1)) * SQ + (m0 & 127) + row) * (DH * 2) + (kt & 1) * 128 + ch * 16, slot + 8192 + (w * 2 + ii) * 1024, 0);
    }
  };
  const int q4 = (lane & 15) >> 2, p4 = lane & 3, blk = (lane >> 4) & 1;
  int tra[2];
#pragma unroll
  for (int t = 0; t < 2; ++t) {
    const int row = 8 * hi + 4 * t + q4;
    const int ch = 4 * wn + 2 * blk + (p4 >> 1);
    tra[t] = 128 * row + 16 * (ch ^ ((row >> 1) & 7)) + 8 * (p4 & 1);
  }
  const int mrow = 32 * wm + r32;
  const int orow = 128 * mrow + 16 * (hi ^ ((mrow >> 1) & 7));
  f32x16 acc = f32x16{};
  constexpr int NKT = HD / K2_KT / K2_SPLIT;
  const int kt0 = bz * NKT;
#pragma unroll
  for (int i = 0; i < K2_NS - 1; ++i) issue(i, i);
  for (int kt = 0; kt < NKT; ++kt) {
    const int buf = kt % K2_NS;
    if (kt + K2_NS - 2 < NKT) wait_vm<4 * (K2_NS - 2)>(); else wait_vm<0>();
    asm volatile("s_waitcnt lgkmcnt(0)" ::: "memory");
    __builtin_amdgcn_s_barrier();
    if (kt + K2_NS - 1 < NKT) issue(kt + K2_NS - 1, (kt + K2_NS - 1) % K2_NS);
    const char* slot = shm + buf * 16384;
    bf16x8 bo[4], aw[4];
#pragma unroll
    for (int ks = 0; ks < 4; ++ks) {
      bo[ks] = *(const bf16x8*)(slot + 8192 + (orow ^ (32 * ks)));
      const s16x4 lo = tr_read(slot + tra[0] + 2048 * ks), hi4 = tr_read(slot + tra[1] + 2048 * ks);
      aw[ks] = __builtin_shufflevector(lo, hi4, 0, 1, 2, 3, 4, 5, 6, 7);
    }
    __builtin_amdgcn_sched_barrier(0);
#pragma unroll
    for (int ks = 0; ks < 4; ++ks) acc = __builtin_amdgcn_mfma_f32_32x32x16_bf16(aw[ks], bo[ks], acc, 0, 0, 0);
  }
  (void)kt0;
  float* dst = part + (size_t)bz * (NB * SQ * DM) + (size_t)(m0 + mrow) * DM + n0 + 32 * wn;
#pragma unroll
  for (int g = 0; g < 4; ++g) {
    typedef float f32x4 __attribute__((ext_vector_type(4)));
    f32x4 v = {acc[4 * g], acc[4 * g + 1], acc[4 * g + 2], acc[4 * g + 3]};
    *(f32x4*)(dst + 8 * g + 4 * hi) = v;
  }
}

__global__ __launch_bounds__(256) void reduce_kernel(const float* __restrict__ part, __hip_bfloat16* __restrict__ out) {
  typedef float f32x4 __attribute__((ext_vector_type(4)));
  const size_t i = ((size_t)blockIdx.x * 256 + threadIdx.x) * 4;
  f32x4 a = *(const f32x4*)(part + i);
#pragma unroll
  for (int z = 1; z < K2_SPLIT; ++z) { const f32x4 t = *(const f32x4*)(part + (size_t)z * (NB * SQ * DM) + i); a += t; }
  u32x2 v = {cvtpk(a[0], a[1]), cvtpk(a[2], a[3])};
  *(u32x2*)((char*)out + i * 2) = v;
}

extern "C" void kernel_launch(void* const* d_in, const int* in_sizes, int n_in,
                              void* d_out, int out_size, void* d_ws, size_t ws_size,
                              hipStream_t stream) {
  const __hip_bfloat16* x  = (const __hip_bfloat16*)d_in[0];
  const __hip_bfloat16* kc = (const __hip_bfloat16*)d_in[1];
  const __hip_bfloat16* vc = (const __hip_bfloat16*)d_in[2];
  const __hip_bfloat16* wq = (const __hip_bfloat16*)d_in[3];
  const __hip_bfloat16* wo = (const __hip_bfloat16*)d_in[4];
  __hip_bfloat16* obuf = (__hip_bfloat16*)d_ws;
  __hip_bfloat16* out = (__hip_bfloat16*)d_out;
  float* part = (float*)((char*)d_ws + (size_t)NB * SQ * HD * 2);
  hipFuncSetAttribute((const void*)attn_fused_kernel, hipFuncAttributeMaxDynamicSharedMemorySize, K1_LDS + 32768);
  hipLaunchKernelGGL(attn_fused_kernel, dim3(NB * NH), dim3(512), K1_LDS + 32768, stream, x, kc, vc, wq, obuf);
  hipFuncSetAttribute((const void*)outproj_kernel, hipFuncAttributeMaxDynamicSharedMemorySize, K2_NS * 16384);
  hipLaunchKernelGGL(outproj_kernel, dim3(16 * 9 * K2_SPLIT), dim3(256), K2_NS * 16384, stream, (const __hip_bfloat16*)obuf, wo, part);
  hipLaunchKernelGGL(reduce_kernel, dim3(NB * SQ * DM / 1024), dim3(256), 0, stream, (const float*)part, out);
}
```

```cpp
#include <hip/hip_runtime.h>
#include <hip/hip_bf16.h>

typedef short    bf16x8 __attribute__((ext_vector_type(8)));
typedef short    s16x4  __attribute__((ext_vector_type(4)));
typedef float    f32x16 __attribute__((ext_vector_type(16)));
typedef float    f32x2  __attribute__((ext_vector_type(2)));
typedef __bf16   bf16x2 __attribute__((ext_vector_type(2)));
typedef unsigned u32x4  __attribute__((ext_vector_type(4)));
typedef unsigned u32x2  __attribute__((ext_vector_type(2)));
typedef __attribute__((address_space(3))) s16x4 lds_s16x4;

constexpr int NB = 8, SQ = 128, DM = 576, NH = 32, SK = 1024, DH = 128, HD = NH * DH;
constexpr int TILE_BYTES = 16384;
constexpr int PAIR_BYTES = 2 * TILE_BYTES;
constexpr int NP = 4;
constexpr int NPA = DM / 64;
constexpr int NPB = SK / 64;
constexpr int NPAIR = NPA + NPB;
constexpr int K1_LDS = NP * PAIR_BYTES;
constexpr float SCALE_L2E = 0.088388347648318447f * 1.4426950408889634f;

#define DI __device__ __forceinline__
DI unsigned cvtpk(float lo, float hi) { f32x2 v = {lo, hi}; bf16x2 b = __builtin_convertvector(v, bf16x2); return __builtin_bit_cast(unsigned, b); }
DI s16x4 tr_read(const char* p) { return __builtin_amdgcn_ds_read_tr16_b64_v4i16((lds_s16x4*)(p)); }
DI void glds_asm(const void* gsrc, const char* lds_dst) {
  unsigned keep; const unsigned d = __builtin_amdgcn_readfirstlane((unsigned)(size_t)lds_dst);
  asm volatile("s_mov_b32 %0, m0\n\ts_mov_b32 m0, %2\n\ts_nop 0\n\tglobal_load_lds_dwordx4 %1, off\n\ts_mov_b32 m0, %0" : "=&s"(keep) : "v"(gsrc), "s"(d) : "memory");
}
DI void glds_asm_nt(const void* gsrc, const char* lds_dst) {
  unsigned keep; const unsigned d = __builtin_amdgcn_readfirstlane((unsigned)(size_t)lds_dst);
  asm volatile("s_mov_b32 %0, m0\n\ts_mov_b32 m0, %2\n\ts_nop 0\n\tglobal_load_lds_dwordx4 %1, off nt\n\ts_mov_b32 m0, %0" : "=&s"(keep) : "v"(gsrc), "s"(d) : "memory");
}
#define GLDS(src, dst, aux) do { if ((aux) == 2) glds_asm_nt((src), (dst)); else glds_asm((src), (dst)); } while (0)

template <int N> DI void wait_vm() { asm volatile("s_waitcnt vmcnt(%0)" ::"n"(N) : "memory"); }

DI bf16x8 pack_nat(const f32x16& a, int base) {
  unsigned a0 = cvtpk(a[base + 0], a[base + 1]), a1 = cvtpk(a[base + 2], a[base + 3]);
  unsigned b0 = cvtpk(a[base + 4], a[base + 5]), b1 = cvtpk(a[base + 6], a[base + 7]);
  auto r0 = __builtin_amdgcn_permlane32_swap(a0, b0, false, false);
  auto r1 = __builtin_amdgcn_permlane32_swap(a1, b1, false, false);
  u32x4 w = {r0[0], r1[0], r0[1], r1[1]};
  return __builtin_bit_cast(bf16x8, w);
}
DI bf16x8 pack_raw(const f32x16& a, int base) {
  u32x4 w = {cvtpk(a[base + 0], a[base + 1]), cvtpk(a[base + 2], a[base + 3]), cvtpk(a[base + 4], a[base + 5]), cvtpk(a[base + 6], a[base + 7])};
  return __builtin_bit_cast(bf16x8, w);
}

__global__ __launch_bounds__(512, 2)
void attn_fused_kernel(const __hip_bfloat16* __restrict__ x, const __hip_bfloat16* __restrict__ kc, const __hip_bfloat16* __restrict__ vc,
                       const __hip_bfloat16* __restrict__ wq, __hip_bfloat16* __restrict__ obuf) {
  extern __shared__ __attribute__((aligned(16))) char shm[];
  const int tid = threadIdx.x, lane = tid & 63, r32 = lane & 31, hi = lane >> 5;
  const int w8 = __builtin_amdgcn_readfirstlane(tid >> 6);
  const int w = w8 & 3, grp = w8 >> 2;
  const int bh = blockIdx.x, b = bh >> 5, h = bh & 31;

  const int l4 = lane >> 4;
  const char* kbase = (const char*)kc + (size_t)bh * SK * DH * 2;
  const char* vbase = (const char*)vc + (size_t)bh * SK * DH * 2;
  const char* wbase = (const char*)wq + (size_t)h * DH * 2;
  const char* xbase = (const char*)x + (size_t)b * SQ * DM * 2;
  auto issue_pair = [&](int pi) {
    char* slot = shm + (pi % NP) * PAIR_BYTES;
#pragma unroll
    for (int ii = 0; ii < 2; ++ii) {
      const int j = w8 * 2 + ii;
      const int row = 4 * j + l4;
      const int ch = (lane & 15) ^ ((l4 << 2) | (j & 3));
      if (pi < NPA) {
        GLDS(wbase + (size_t)(pi * 64 + row) * (HD * 2) + ch * 16, slot + j * 1024, 0);
        const int xr = 8 * j + (lane >> 3);
        const int xc = (lane & 7) ^ ((j & 1) * 4 + l4);
        GLDS(xbase + (size_t)xr * (DM * 2) + pi * 128 + xc * 16, slot + TILE_BYTES + j * 1024, 0);
      } else {
        const int t = pi - NPA;
        GLDS(kbase + (size_t)t * TILE_BYTES + row * 256 + ch * 16, slot + j * 1024, 2);
        GLDS(vbase + (size_t)t * TILE_BYTES + row * 256 + ch * 16, slot + TILE_BYTES + j * 1024, 2);
      }
    }
  };
  int nxt = 0;
  for (; nxt < NP - 1; ++nxt) issue_pair(nxt);

  const int q4 = (lane & 15) >> 2, p4 = lane & 3, blk = (lane >> 4) & 1;
  int trn[2][2], trp[2][4];
#pragma unroll
  for (int t = 0; t < 2; ++t) {
#pragma unroll
    for (int c = 0; c < 4; ++c) {
      const int ch = 4 * c + 2 * blk + (p4 >> 1);
      const int row = 8 * t + 4 * hi + q4; const int sw = ((row & 3) << 2) | ((row >> 2) & 3);
      trp[t][c] = 256 * row + 16 * (ch ^ sw) + 8 * (p4 & 1);
    }
#pragma unroll
    for (int cc = 0; cc < 2; ++cc) {
      const int ch = 4 * (2 * grp + cc) + 2 * blk + (p4 >> 1);
      const int row = 8 * hi + 4 * t + q4; const int sw = ((row & 3) << 2) | ((row >> 2) & 3);
      trn[t][cc] = 256 * row + 16 * (ch ^ sw) + 8 * (p4 & 1);
    }
  }
  const int swr = ((r32 & 3) << 2) | ((r32 >> 2) & 3);
  const int krow = 256 * r32 + 16 * (hi ^ swr);
  const int xq = 32 * w + r32;
  const int xrow = 128 * xq + 16 * (hi ^ ((xq >> 1) & 7));

  f32x16 acc[2] = {f32x16{}, f32x16{}};
  for (int pi = 0; pi < NPA; ++pi) {
    wait_vm<8>();
    asm volatile("s_waitcnt lgkmcnt(0)" ::: "memory");
    __builtin_amdgcn_s_barrier();
    if (nxt < NPAIR) { issue_pair(nxt); ++nxt; }
    const char* slot = shm + (pi % NP) * PAIR_BYTES;
    bf16x8 bx[4], aw[4][2];
#pragma unroll
    for (int ks = 0; ks < 4; ++ks) {
      bx[ks] = *(const bf16x8*)(slot + TILE_BYTES + (xrow ^ (32 * ks)));
#pragma unroll
      for (int cc = 0; cc < 2; ++cc) {
        const s16x4 lo = tr_read(slot + trn[0][cc] + 4096 * ks), hi4 = tr_read(slot + trn[1][cc] + 4096 * ks);
        aw[ks][cc] = __builtin_shufflevector(lo, hi4, 0, 1, 2, 3, 4, 5, 6, 7);
      }
    }
    __builtin_amdgcn_sched_barrier(0);
#pragma unroll
    for (int ks = 0; ks < 4; ++ks)
#pragma unroll
      for (int cc = 0; cc < 2; ++cc) acc[cc] = __builtin_amdgcn_mfma_f32_32x32x16_bf16(aw[ks][cc], bx[ks], acc[cc], 0, 0, 0);
  }
  bf16x8 qf[8];
  {
    char* xch = shm + K1_LDS;
#pragma unroll
    for (int cc = 0; cc < 2; ++cc) {
      qf[4 * grp + 2 * cc] = pack_nat(acc[cc], 0); qf[4 * grp + 2 * cc + 1] = pack_nat(acc[cc], 8);
      *(bf16x8*)(xch + ((w * 8 + 4 * grp + 2 * cc) * 64 + lane) * 16) = qf[4 * grp + 2 * cc];
      *(bf16x8*)(xch + ((w * 8 + 4 * grp + 2 * cc + 1) * 64 + lane) * 16) = qf[4 * grp + 2 * cc + 1];
    }
    asm volatile("s_waitcnt lgkmcnt(0)" ::: "memory");
    __builtin_amdgcn_s_barrier();
#pragma unroll
    for (int s = 0; s < 8; ++s) qf[s] = *(const bf16x8*)(xch + ((w * 8 + s) * 64 + lane) * 16);
  }

  f32x16 o[4];
#pragma unroll
  for (int c = 0; c < 4; ++c) o[c] = f32x16{};
  float m_run = -__builtin_inff(), l_run = 0.f;
  for (int P = NPA; P < NPAIR; P += 2) {
    wait_vm<0>();
    asm volatile("s_waitcnt lgkmcnt(0)" ::: "memory");
    __builtin_amdgcn_s_barrier();
    while (nxt < NPAIR && nxt < P + NP) { issue_pair(nxt); ++nxt; }
    const char* slot = shm + ((P + grp) % NP) * PAIR_BYTES;
    f32x16 p0 = f32x16{}, p1 = f32x16{};
    bf16x8 kf0[8], kf1[8];
#pragma unroll
    for (int s = 0; s < 8; ++s) {
      kf0[s] = *(const bf16x8*)(slot + (krow ^ (32 * s)));
      kf1[s] = *(const bf16x8*)(slot + 8192 + (krow ^ (32 * s)));
    }
    __builtin_amdgcn_sched_barrier(0);
#pragma unroll
    for (int s = 0; s < 8; ++s) {
      p0 = __builtin_amdgcn_mfma_f32_32x32x16_bf16(kf0[s], qf[s], p0, 0, 0, 0);
      p1 = __builtin_amdgcn_mfma_f32_32x32x16_bf16(kf1[s], qf[s], p1, 0, 0, 0);
    }
    __builtin_amdgcn_sched_barrier(0);
    bf16x8 av[4][4];
    { const char* vs = slot + TILE_BYTES;
#pragma unroll
      for (int c = 0; c < 2; ++c)
#pragma unroll
        for (int ks = 0; ks < 4; ++ks) {
          const s16x4 lo = tr_read(vs + trp[0][c] + 4096 * ks), hi4 = tr_read(vs + trp[1][c] + 4096 * ks);
          av[c][ks] = __builtin_shufflevector(lo, hi4, 0, 1, 2, 3, 4, 5, 6, 7);
        } }
    __builtin_amdgcn_sched_barrier(0);
    float mx = p0[0];
#pragma unroll
    for (int r = 1; r < 16; ++r) mx = fmaxf(mx, p0[r]);
#pragma unroll
    for (int r = 0; r < 16; ++r) mx = fmaxf(mx, p1[r]);
    { auto rr = __builtin_amdgcn_permlane32_swap(__float_as_uint(mx), __float_as_uint(mx), false, false);
      mx = fmaxf(__uint_as_float(rr[0]), __uint_as_float(rr[1])); }
    const float m_new = fmaxf(m_run, mx);
    if (__any(m_new > m_run)) {
      const float alpha = __builtin_amdgcn_exp2f((m_run - m_new) * SCALE_L2E);
#pragma unroll
      for (int c = 0; c < 4; ++c)
#pragma unroll
        for (int r = 0; r < 16; ++r) o[c][r] *= alpha;
      l_run *= alpha;
      m_run = m_new;
    }
    const float nm = -m_run * SCALE_L2E;
    float ps = 0.f;
#pragma unroll
    for (int r = 0; r < 16; ++r) { p0[r] = __builtin_amdgcn_exp2f(fmaf(p0[r], SCALE_L2E, nm)); ps += p0[r]; }
#pragma unroll
    for (int r = 0; r < 16; ++r) { p1[r] = __builtin_amdgcn_exp2f(fmaf(p1[r], SCALE_L2E, nm)); ps += p1[r]; }
    l_run += ps;
    bf16x8 pb[4];
    pb[0] = pack_raw(p0, 0); pb[1] = pack_raw(p0, 8); pb[2] = pack_raw(p1, 0); pb[3] = pack_raw(p1, 8);
    __builtin_amdgcn_sched_barrier(0);
    { const char* vs = slot + TILE_BYTES;
#pragma unroll
      for (int c = 2; c < 4; ++c)
#pragma unroll
        for (int ks = 0; ks < 4; ++ks) {
          const s16x4 lo = tr_read(vs + trp[0][c] + 4096 * ks), hi4 = tr_read(vs + trp[1][c] + 4096 * ks);
          av[c][ks] = __builtin_shufflevector(lo, hi4, 0, 1, 2, 3, 4, 5, 6, 7);
        } }
    __builtin_amdgcn_sched_barrier(0);
#pragma unroll
    for (int c = 0; c < 4; ++c)
#pragma unroll
      for (int ks = 0; ks < 4; ++ks) o[c] = __builtin_amdgcn_mfma_f32_32x32x16_bf16(av[c][ks], pb[ks], o[c], 0, 0, 0);
  }
  { auto rr = __builtin_amdgcn_permlane32_swap(__float_as_uint(l_run), __float_as_uint(l_run), false, false);
    l_run = __uint_as_float(rr[0]) + __uint_as_float(rr[1]); }
  asm volatile("s_waitcnt lgkmcnt(0)" ::: "memory");
  __builtin_amdgcn_s_barrier();
  float* mg = (float*)shm + w * (66 * 64);
  if (grp == 1) {
#pragma unroll
    for (int c = 0; c < 4; ++c)
#pragma unroll
      for (int r = 0; r < 16; ++r) mg[(c * 16 + r) * 64 + lane] = o[c][r];
    mg[64 * 64 + lane] = m_run; mg[65 * 64 + lane] = l_run;
  }
  asm volatile("s_waitcnt lgkmcnt(0)" ::: "memory");
  __builtin_amdgcn_s_barrier();
  if (grp == 0) {
    const float m1 = mg[64 * 64 + lane], l1 = mg[65 * 64 + lane];
    const float mm = fmaxf(m_run, m1);
    const float a0 = __builtin_amdgcn_exp2f((m_run - mm) * SCALE_L2E), a1 = __builtin_amdgcn_exp2f((m1 - mm) * SCALE_L2E);
    const float inv = 1.f / (l_run * a0 + l1 * a1);
    const float s0 = a0 * inv, s1 = a1 * inv;
    char* orow = (char*)obuf + ((size_t)bh * SQ + 32 * w + r32) * (DH * 2);
#pragma unroll
    for (int c = 0; c < 4; ++c)
#pragma unroll
      for (int g = 0; g < 4; ++g) {
        float v[4];
#pragma unroll
        for (int e = 0; e < 4; ++e) v[e] = o[c][4 * g + e] * s0 + mg[(c * 16 + 4 * g + e) * 64 + lane] * s1;
        u32x2 pk = {cvtpk(v[0], v[1]), cvtpk(v[2], v[3])};
        *(u32x2*)(orow + (32 * c + 8 * g + 4 * hi) * 2) = pk;
      }
  }
}

#define K2NS 3
constexpr int K2_KT = 64, K2_NS = K2NS, K2_SPLIT = 8, K2_STAGE = 40960;
__global__ __launch_bounds__(512, 2)
void outproj_kernel(const __hip_bfloat16* __restrict__ obuf, const __hip_bfloat16* __restrict__ wo, float* __restrict__ part) {
  extern __shared__ __attribute__((aligned(16))) char shm[];
  const int tid = threadIdx.x, lane = tid & 63, r32 = lane & 31, hi = lane >> 5;
  const int w8 = __builtin_amdgcn_readfirstlane(tid >> 6);
  const int wm = w8 & 3, wn = w8 >> 2;
  const int bz = blockIdx.x & 7, tile = blockIdx.x >> 3;
  const int bt = tile & 7, n0 = (tile >> 3) * 192;
  const int l3 = lane >> 3;
  constexpr int NKT = HD / K2_KT / K2_SPLIT;
  auto issue = [&](int kt, int buf) {
    kt += bz * NKT;
    char* slot = shm + buf * K2_STAGE;
#pragma unroll
    for (int i = 0; i < 5; ++i) {
      const int id = w8 * 5 + i, st = id >> 3, pc = id & 7;
      const int row = pc * 8 + l3;
      const int ch = (lane & 7) ^ ((row >> 1) & 7);
      if (st < 3) GLDS((const char*)wo + ((size_t)(kt * 64 + row) * DM + n0 + st * 64) * 2 + ch * 16, slot + id * 1024, 0);
      else GLDS((const char*)obuf + ((size_t)(bt * NH + (kt >> 1)) * SQ + (st - 3) * 64 + row) * (DH * 2) + (kt & 1) * 128 + ch * 16, slot + id * 1024, 0);
    }
  };
  const int q4 = (lane & 15) >> 2, p4 = lane & 3, blk = (lane >> 4) & 1;
  int tra[3][2];
#pragma unroll
  for (int i = 0; i < 3; ++i)
#pragma unroll
    for (int t = 0; t < 2; ++t) {
      const int nb = 3 * wn + i;
      const int row = 8 * hi + 4 * t + q4;
      const int ch = 4 * (nb & 1) + 2 * blk + (p4 >> 1);
      tra[i][t] = (nb >> 1) * 8192 + 128 * row + 16 * (ch ^ ((row >> 1) & 7)) + 8 * (p4 & 1);
    }
  const int mr = 32 * (wm & 1) + r32;
  const int orow = (3 + (wm >> 1)) * 8192 + 128 * mr + 16 * (hi ^ ((mr >> 1) & 7));
  f32x16 acc[3] = {f32x16{}, f32x16{}, f32x16{}};
#pragma unroll
  for (int i = 0; i < K2_NS - 1; ++i) issue(i, i);
  for (int kt = 0; kt < NKT; ++kt) {
    const int buf = kt % K2_NS;
    if (kt + K2_NS - 2 < NKT) wait_vm<5 * (K2_NS - 2)>(); else wait_vm<0>();
    asm volatile("s_waitcnt lgkmcnt(0)" ::: "memory");
    __builtin_amdgcn_s_barrier();
    if (kt + K2_NS - 1 < NKT) issue(kt + K2_NS - 1, (kt + K2_NS - 1) % K2_NS);
    const char* slot = shm + buf * K2_STAGE;
    bf16x8 bo[4], aw[4][3];
#pragma unroll
    for (int ks = 0; ks < 4; ++ks) {
      bo[ks] = *(const bf16x8*)(slot + (orow ^ (32 * ks)));
#pragma unroll
      for (int i = 0; i < 3; ++i) {
        const s16x4 lo = tr_read(slot + tra[i][0] + 2048 * ks), hi4 = tr_read(slot + tra[i][1] + 2048 * ks);
        aw[ks][i] = __builtin_shufflevector(lo, hi4, 0, 1, 2, 3, 4, 5, 6, 7);
      }
    }
    __builtin_amdgcn_sched_barrier(0);
#pragma unroll
    for (int ks = 0; ks < 4; ++ks)
#pragma unroll
      for (int i = 0; i < 3; ++i) acc[i] = __builtin_amdgcn_mfma_f32_32x32x16_bf16(aw[ks][i], bo[ks], acc[i], 0, 0, 0);
  }
  float* dst = part + (size_t)bz * (NB * SQ * DM) + (size_t)(bt * SQ + 32 * wm + r32) * DM + n0 + 96 * wn;
#pragma unroll
  for (int i = 0; i < 3; ++i)
#pragma unroll
    for (int g = 0; g < 4; ++g) {
      typedef float f32x4 __attribute__((ext_vector_type(4)));
      f32x4 v = {acc[i][4 * g], acc[i][4 * g + 1], acc[i][4 * g + 2], acc[i][4 * g + 3]};
      *(f32x4*)(dst + 32 * i + 8 * g + 4 * hi) = v;
    }
}

__global__ __launch_bounds__(256) void reduce_kernel(const float* __restrict__ part, __hip_bfloat16* __restrict__ out) {
  typedef float f32x4 __attribute__((ext_vector_type(4)));
  const size_t i = ((size_t)blockIdx.x * 256 + threadIdx.x) * 4;
  f32x4 a = *(const f32x4*)(part + i);
#pragma unroll
  for (int z = 1; z < K2_SPLIT; ++z) { const f32x4 t = *(const f32x4*)(part + (size_t)z * (NB * SQ * DM) + i); a += t; }
  u32x2 v = {cvtpk(a[0], a[1]), cvtpk(a[2], a[3])};
  *(u32x2*)((char*)out + i * 2) = v;
}

extern "C" void kernel_launch(void* const* d_in, const int* in_sizes, int n_in,
                              void* d_out, int out_size, void* d_ws, size_t ws_size,
                              hipStream_t stream) {
  const __hip_bfloat16* x  = (const __hip_bfloat16*)d_in[0];
  const __hip_bfloat16* kc = (const __hip_bfloat16*)d_in[1];
  const __hip_bfloat16* vc = (const __hip_bfloat16*)d_in[2];
  const __hip_bfloat16* wq = (const __hip_bfloat16*)d_in[3];
  const __hip_bfloat16* wo = (const __hip_bfloat16*)d_in[4];
  __hip_bfloat16* obuf = (__hip_bfloat16*)d_ws;
  __hip_bfloat16* out = (__hip_bfloat16*)d_out;
  float* part = (float*)((char*)d_ws + (size_t)NB * SQ * HD * 2);
  hipFuncSetAttribute((const void*)attn_fused_kernel, hipFuncAttributeMaxDynamicSharedMemorySize, K1_LDS + 32768);
  hipLaunchKernelGGL(attn_fused_kernel, dim3(NB * NH), dim3(512), K1_LDS + 32768, stream, x, kc, vc, wq, obuf);
  hipFuncSetAttribute((const void*)outproj_kernel, hipFuncAttributeMaxDynamicSharedMemorySize, K2_NS * K2_STAGE);
  hipLaunchKernelGGL(outproj_kernel, dim3(8 * 3 * K2_SPLIT), dim3(512), K2_NS * K2_STAGE, stream, (const __hip_bfloat16*)obuf, wo, part);
  hipLaunchKernelGGL(reduce_kernel, dim3(NB * SQ * DM / 1024), dim3(256), 0, stream, (const float*)part, out);
}
```

```cpp
#include <hip/hip_runtime.h>
#include <hip/hip_bf16.h>

typedef short    bf16x8 __attribute__((ext_vector_type(8)));
typedef short    s16x4  __attribute__((ext_vector_type(4)));
typedef float    f32x16 __attribute__((ext_vector_type(16)));
typedef float    f32x2  __attribute__((ext_vector_type(2)));
typedef __bf16   bf16x2 __attribute__((ext_vector_type(2)));
typedef unsigned u32x4  __attribute__((ext_vector_type(4)));
typedef unsigned u32x2  __attribute__((ext_vector_type(2)));
typedef __attribute__((address_space(3))) s16x4 lds_s16x4;

constexpr int NB = 8, SQ = 128, DM = 576, NH = 32, SK = 1024, DH = 128, HD = NH * DH;
constexpr int TILE_BYTES = 16384;
constexpr int PAIR_BYTES = 2 * TILE_BYTES;
constexpr int NP = 5;
constexpr int NPA = DM / 64;
constexpr int NPB = SK / 64;
constexpr int NPAIR = NPA + NPB;
constexpr int K1_LDS = NP * PAIR_BYTES;
constexpr float SCALE_L2E = 0.088388347648318447f * 1.4426950408889634f;

#define DI __device__ __forceinline__
DI unsigned cvtpk(float lo, float hi) { f32x2 v = {lo, hi}; bf16x2 b = __builtin_convertvector(v, bf16x2); return __builtin_bit_cast(unsigned, b); }
DI s16x4 tr_read(const char* p) { return __builtin_amdgcn_ds_read_tr16_b64_v4i16((lds_s16x4*)(p)); }
DI void glds_asm(const void* gsrc, const char* lds_dst) {
  unsigned keep; const unsigned d = __builtin_amdgcn_readfirstlane((unsigned)(size_t)lds_dst);
  asm volatile("s_mov_b32 %0, m0\n\ts_mov_b32 m0, %2\n\ts_nop 0\n\tglobal_load_lds_dwordx4 %1, off\n\ts_mov_b32 m0, %0" : "=&s"(keep) : "v"(gsrc), "s"(d) : "memory");
}
DI void glds_asm_nt(const void* gsrc, const char* lds_dst) {
  unsigned keep; const unsigned d = __builtin_amdgcn_readfirstlane((unsigned)(size_t)lds_dst);
  asm volatile("s_mov_b32 %0, m0\n\ts_mov_b32 m0, %2\n\ts_nop 0\n\tglobal_load_lds_dwordx4 %1, off nt\n\ts_mov_b32 m0, %0" : "=&s"(keep) : "v"(gsrc), "s"(d) : "memory");
}
#define GLDS(src, dst, aux) do { if ((aux) == 2) glds_asm_nt((src), (dst)); else glds_asm((src), (dst)); } while (0)

template <int N> DI void wait_vm() { asm volatile("s_waitcnt vmcnt(%0)" ::"n"(N) : "memory"); }

DI bf16x8 pack_nat(const f32x16& a, int base) {
  unsigned a0 = cvtpk(a[base + 0], a[base + 1]), a1 = cvtpk(a[base + 2], a[base + 3]);
  unsigned b0 = cvtpk(a[base + 4], a[base + 5]), b1 = cvtpk(a[base + 6], a[base + 7]);
  auto r0 = __builtin_amdgcn_permlane32_swap(a0, b0, false, false);
  auto r1 = __builtin_amdgcn_permlane32_swap(a1, b1, false, false);
  u32x4 w = {r0[0], r1[0], r0[1], r1[1]};
  return __builtin_bit_cast(bf16x8, w);
}
DI bf16x8 pack_raw(const f32x16& a, int base) {
  u32x4 w = {cvtpk(a[base + 0], a[base + 1]), cvtpk(a[base + 2], a[base + 3]), cvtpk(a[base + 4], a[base + 5]), cvtpk(a[base + 6], a[base + 7])};
  return __builtin_bit_cast(bf16x8, w);
}

__global__ __launch_bounds__(512, 2)
void attn_fused_kernel(const __hip_bfloat16* __restrict__ x, const __hip_bfloat16* __restrict__ kc, const __hip_bfloat16* __restrict__ vc,
                       const __hip_bfloat16* __restrict__ wq, __hip_bfloat16* __restrict__ obuf) {
  extern __shared__ __attribute__((aligned(16))) char shm[];
  const int tid = threadIdx.x, lane = tid & 63, r32 = lane & 31, hi = lane >> 5;
  const int w8 = __builtin_amdgcn_readfirstlane(tid >> 6);
  const int w = w8 & 3, grp = w8 >> 2;
  const int bh = blockIdx.x, b = bh >> 5, h = bh & 31;

  const int l4 = lane >> 4;
  const char* kbase = (const char*)kc + (size_t)bh * SK * DH * 2;
  const char* vbase = (const char*)vc + (size_t)bh * SK * DH * 2;
  const char* wbase = (const char*)wq + (size_t)h * DH * 2;
  const char* xbase = (const char*)x + (size_t)b * SQ * DM * 2;
  auto issue_pair = [&](int pi) {
    char* slot = shm + (pi % NP) * PAIR_BYTES;
#pragma unroll
    for (int ii = 0; ii < 2; ++ii) {
      const int j = w8 * 2 + ii;
      const int row = 4 * j + l4;
      const int ch = (lane & 15) ^ ((l4 << 2) | (j & 3));
      if (pi < NPA) {
        GLDS(wbase + (size_t)(pi * 64 + row) * (HD * 2) + ch * 16, slot + j * 1024, 0);
        const int xr = 8 * j + (lane >> 3);
        const int xc = (lane & 7) ^ ((j & 1) * 4 + l4);
        GLDS(xbase + (size_t)xr * (DM * 2) + pi * 128 + xc * 16, slot + TILE_BYTES + j * 1024, 0);
      } else {
        const int t = pi - NPA;
        GLDS(kbase + (size_t)t * TILE_BYTES + row * 256 + ch * 16, slot + j * 1024, 2);
        GLDS(vbase + (size_t)t * TILE_BYTES + row * 256 + ch * 16, slot + TILE_BYTES + j * 1024, 2);
      }
    }
  };
  int nxt = 0;
  for (; nxt < NP - 1; ++nxt) issue_pair(nxt);

  const int q4 = (lane & 15) >> 2, p4 = lane & 3, blk = (lane >> 4) & 1;
  int trn[2][2], trp[2][4];
#pragma unroll
  for (int t = 0; t < 2; ++t) {
#pragma unroll
    for (int c = 0; c < 4; ++c) {
      const int ch = 4 * c + 2 * blk + (p4 >> 1);
      const int row = 8 * t + 4 * hi + q4; const int sw = ((row & 3) << 2) | ((row >> 2) & 3);
      trp[t][c] = 256 * row + 16 * (ch ^ sw) + 8 * (p4 & 1);
    }
#pragma unroll
    for (int cc = 0; cc < 2; ++cc) {
      const int ch = 4 * (2 * grp + cc) + 2 * blk + (p4 >> 1);
      const int row = 8 * hi + 4 * t + q4; const int sw = ((row & 3) << 2) | ((row >> 2) & 3);
      trn[t][cc] = 256 * row + 16 * (ch ^ sw) + 8 * (p4 & 1);
    }
  }
  const int swr = ((r32 & 3) << 2) | ((r32 >> 2) & 3);
  const int krow = 256 * r32 + 16 * (hi ^ swr);
  const int xq = 32 * w + r32;
  const int xrow = 128 * xq + 16 * (hi ^ ((xq >> 1) & 7));

  f32x16 acc[2] = {f32x16{}, f32x16{}};
  for (int pi = 0; pi < NPA; ++pi) {
    wait_vm<4 * (NP - 2)>();
    asm volatile("s_waitcnt lgkmcnt(0)" ::: "memory");
    __builtin_amdgcn_s_barrier();
    if (nxt < NPAIR) { issue_pair(nxt); ++nxt; }
    const char* slot = shm + (pi % NP) * PAIR_BYTES;
    bf16x8 bx[4], aw[4][2];
#pragma unroll
    for (int ks = 0; ks < 4; ++ks) {
      bx[ks] = *(const bf16x8*)(slot + TILE_BYTES + (xrow ^ (32 * ks)));
#pragma unroll
      for (int cc = 0; cc < 2; ++cc) {
        const s16x4 lo = tr_read(slot + trn[0][cc] + 4096 * ks), hi4 = tr_read(slot + trn[1][cc] + 4096 * ks);
        aw[ks][cc] = __builtin_shufflevector(lo, hi4, 0, 1, 2, 3, 4, 5, 6, 7);
      }
    }
    __builtin_amdgcn_sched_barrier(0);
#pragma unroll
    for (int ks = 0; ks < 4; ++ks)
#pragma unroll
      for (int cc = 0; cc < 2; ++cc) acc[cc] = __builtin_amdgcn_mfma_f32_32x32x16_bf16(aw[ks][cc], bx[ks], acc[cc], 0, 0, 0);
  }
  bf16x8 qf[8];
  {
    char* xch = shm + ((NPA - 1) % NP) * PAIR_BYTES;
    asm volatile("s_waitcnt lgkmcnt(0)" ::: "memory");
    __builtin_amdgcn_s_barrier();
#pragma unroll
    for (int cc = 0; cc < 2; ++cc) {
      qf[4 * grp + 2 * cc] = pack_nat(acc[cc], 0); qf[4 * grp + 2 * cc + 1] = pack_nat(acc[cc], 8);
      *(bf16x8*)(xch + ((w * 8 + 4 * grp + 2 * cc) * 64 + lane) * 16) = qf[4 * grp + 2 * cc];
      *(bf16x8*)(xch + ((w * 8 + 4 * grp + 2 * cc + 1) * 64 + lane) * 16) = qf[4 * grp + 2 * cc + 1];
    }
    asm volatile("s_waitcnt lgkmcnt(0)" ::: "memory");
    __builtin_amdgcn_s_barrier();
#pragma unroll
    for (int s = 0; s < 8; ++s) qf[s] = *(const bf16x8*)(xch + ((w * 8 + s) * 64 + lane) * 16);
  }

  f32x16 o[4];
#pragma unroll
  for (int c = 0; c < 4; ++c) o[c] = f32x16{};
  float m_run = -__builtin_inff(), l_run = 0.f;
  for (int P = NPA; P < NPAIR; P += 2) {
    if (P + 2 < NPAIR) wait_vm<4>(); else wait_vm<0>();
    asm volatile("s_waitcnt lgkmcnt(0)" ::: "memory");
    __builtin_amdgcn_s_barrier();
    while (nxt < NPAIR && nxt < P + NP) { issue_pair(nxt); ++nxt; }
    const char* slot = shm + ((P + grp) % NP) * PAIR_BYTES;
    f32x16 p0 = f32x16{}, p1 = f32x16{};
    bf16x8 kf0[8], kf1[8];
#pragma unroll
    for (int s = 0; s < 8; ++s) {
      kf0[s] = *(const bf16x8*)(slot + (krow ^ (32 * s)));
      kf1[s] = *(const bf16x8*)(slot + 8192 + (krow ^ (32 * s)));
    }
    __builtin_amdgcn_sched_barrier(0);
#pragma unroll
    for (int s = 0; s < 8; ++s) {
      p0 = __builtin_amdgcn_mfma_f32_32x32x16_bf16(kf0[s], qf[s], p0, 0, 0, 0);
      p1 = __builtin_amdgcn_mfma_f32_32x32x16_bf16(kf1[s], qf[s], p1, 0, 0, 0);
    }
    __builtin_amdgcn_sched_barrier(0);
    bf16x8 av[4][4];
    { const char* vs = slot + TILE_BYTES;
#pragma unroll
      for (int c = 0; c < 2; ++c)
#pragma unroll
        for (int ks = 0; ks < 4; ++ks) {
          const s16x4 lo = tr_read(vs + trp[0][c] + 4096 * ks), hi4 = tr_read(vs + trp[1][c] + 4096 * ks);
          av[c][ks] = __builtin_shufflevector(lo, hi4, 0, 1, 2, 3, 4, 5, 6, 7);
        } }
    __builtin_amdgcn_sched_barrier(0);
    float mx = p0[0];
#pragma unroll
    for (int r = 1; r < 16; ++r) mx = fmaxf(mx, p0[r]);
#pragma unroll
    for (int r = 0; r < 16; ++r) mx = fmaxf(mx, p1[r]);
    { auto rr = __builtin_amdgcn_permlane32_swap(__float_as_uint(mx), __float_as_uint(mx), false, false);
      mx = fmaxf(__uint_as_float(rr[0]), __uint_as_float(rr[1])); }
    const float m_new = fmaxf(m_run, mx);
    if (__any(m_new > m_run)) {
      const float alpha = __builtin_amdgcn_exp2f((m_run - m_new) * SCALE_L2E);
#pragma unroll
      for (int c = 0; c < 4; ++c)
#pragma unroll
        for (int r = 0; r < 16; ++r) o[c][r] *= alpha;
      l_run *= alpha;
      m_run = m_new;
    }
    const float nm = -m_run * SCALE_L2E;
    float ps = 0.f;
#pragma unroll
    for (int r = 0; r < 16; ++r) { p0[r] = __builtin_amdgcn_exp2f(fmaf(p0[r], SCALE_L2E, nm)); ps += p0[r]; }
#pragma unroll
    for (int r = 0; r < 16; ++r) { p1[r] = __builtin_amdgcn_exp2f(fmaf(p1[r], SCALE_L2E, nm)); ps += p1[r]; }
    l_run += ps;
    bf16x8 pb[4];
    pb[0] = pack_raw(p0, 0); pb[1] = pack_raw(p0, 8); pb[2] = pack_raw(p1, 0); pb[3] = pack_raw(p1, 8);
    __builtin_amdgcn_sched_barrier(0);
    { const char* vs = slot + TILE_BYTES;
#pragma unroll
      for (int c = 2; c < 4; ++c)
#pragma unroll
        for (int ks = 0; ks < 4; ++ks) {
          const s16x4 lo = tr_read(vs + trp[0][c] + 4096 * ks), hi4 = tr_read(vs + trp[1][c] + 4096 * ks);
          av[c][ks] = __builtin_shufflevector(lo, hi4, 0, 1, 2, 3, 4, 5, 6, 7);
        } }
    __builtin_amdgcn_sched_barrier(0);
#pragma unroll
    for (int c = 0; c < 4; ++c)
#pragma unroll
      for (int ks = 0; ks < 4; ++ks) o[c] = __builtin_amdgcn_mfma_f32_32x32x16_bf16(av[c][ks], pb[ks], o[c], 0, 0, 0);
  }
  { auto rr = __builtin_amdgcn_permlane32_swap(__float_as_uint(l_run), __float_as_uint(l_run), false, false);
    l_run = __uint_as_float(rr[0]) + __uint_as_float(rr[1]); }
  asm volatile("s_waitcnt lgkmcnt(0)" ::: "memory");
  __builtin_amdgcn_s_barrier();
  float* mg = (float*)shm + w * (66 * 64);
  if (grp == 1) {
#pragma unroll
    for (int c = 0; c < 4; ++c)
#pragma unroll
      for (int r = 0; r < 16; ++r) mg[(c * 16 + r) * 64 + lane] = o[c][r];
    mg[64 * 64 + lane] = m_run; mg[65 * 64 + lane] = l_run;
  }
  asm volatile("s_waitcnt lgkmcnt(0)" ::: "memory");
  __builtin_amdgcn_s_barrier();
  if (grp == 0) {
    const float m1 = mg[64 * 64 + lane], l1 = mg[65 * 64 + lane];
    const float mm = fmaxf(m_run, m1);
    const float a0 = __builtin_amdgcn_exp2f((m_run - mm) * SCALE_L2E), a1 = __builtin_amdgcn_exp2f((m1 - mm) * SCALE_L2E);
    const float inv = 1.f / (l_run * a0 + l1 * a1);
    const float s0 = a0 * inv, s1 = a1 * inv;
    char* orow = (char*)obuf + ((size_t)bh * SQ + 32 * w + r32) * (DH * 2);
#pragma unroll
    for (int c = 0; c < 4; ++c)
#pragma unroll
      for (int g = 0; g < 4; ++g) {
        float v[4];
#pragma unroll
        for (int e = 0; e < 4; ++e) v[e] = o[c][4 * g + e] * s0 + mg[(c * 16 + 4 * g + e) * 64 + lane] * s1;
        u32x2 pk = {cvtpk(v[0], v[1]), cvtpk(v[2], v[3])};
        *(u32x2*)(orow + (32 * c + 8 * g + 4 * hi) * 2) = pk;
      }
  }
}

#define K2NS 3
constexpr int K2_KT = 64, K2_NS = K2NS, K2_SPLIT = 8, K2_STAGE = 40960;
__global__ __launch_bounds__(512, 2)
void outproj_kernel(const __hip_bfloat16* __restrict__ obuf, const __hip_bfloat16* __restrict__ wo, float* __restrict__ part) {
  extern __shared__ __attribute__((aligned(16))) char shm[];
  const int tid = threadIdx.x, lane = tid & 63, r32 = lane & 31, hi = lane >> 5;
  const int w8 = __builtin_amdgcn_readfirstlane(tid >> 6);
  const int wm = w8 & 3, wn = w8 >> 2;
  const int bz = blockIdx.x & 7, tile = blockIdx.x >> 3;
  const int bt = tile & 7, n0 = (tile >> 3) * 192;
  const int l3 = lane >> 3;
  constexpr int NKT = HD / K2_KT / K2_SPLIT;
  auto issue = [&](int kt, int buf) {
    kt += bz * NKT;
    char* slot = shm + buf * K2_STAGE;
#pragma unroll
    for (int i = 0; i < 5; ++i) {
      const int id = w8 * 5 + i, st = id >> 3, pc = id & 7;
      const int row = pc * 8 + l3;
      const int ch = (lane & 7) ^ ((row >> 1) & 7);
      if (st < 3) GLDS((const char*)wo + ((size_t)(kt * 64 + row) * DM + n0 + st * 64) * 2 + ch * 16, slot + id * 1024, 0);
      else GLDS((const char*)obuf + ((size_t)(bt * NH + (kt >> 1)) * SQ + (st - 3) * 64 + row) * (DH * 2) + (kt & 1) * 128 + ch * 16, slot + id * 1024, 0);
    }
  };
  const int q4 = (lane & 15) >> 2, p4 = lane & 3, blk = (lane >> 4) & 1;
  int tra[3][2];
#pragma unroll
  for (int i = 0; i < 3; ++i)
#pragma unroll
    for (int t = 0; t < 2; ++t) {
      const int nb = 3 * wn + i;
      const int row = 8 * hi + 4 * t + q4;
      const int ch = 4 * (nb & 1) + 2 * blk + (p4 >> 1);
      tra[i][t] = (nb >> 1) * 8192 + 128 * row + 16 * (ch ^ ((row >> 1) & 7)) + 8 * (p4 & 1);
    }
  const int mr = 32 * (wm & 1) + r32;
  const int orow = (3 + (wm >> 1)) * 8192 + 128 * mr + 16 * (hi ^ ((mr >> 1) & 7));
  f32x16 acc[3] = {f32x16{}, f32x16{}, f32x16{}};
#pragma unroll
  for (int i = 0; i < K2_NS - 1; ++i) issue(i, i);
  for (int kt = 0; kt < NKT; ++kt) {
    const int buf = kt % K2_NS;
    if (kt + K2_NS - 2 < NKT) wait_vm<5 * (K2_NS - 2)>(); else wait_vm<0>();
    asm volatile("s_waitcnt lgkmcnt(0)" ::: "memory");
    __builtin_amdgcn_s_barrier();
    if (kt + K2_NS - 1 < NKT) issue(kt + K2_NS - 1, (kt + K2_NS - 1) % K2_NS);
    const char* slot = shm + buf * K2_STAGE;
    bf16x8 bo[4], aw[4][3];
#pragma unroll
    for (int ks = 0; ks < 4; ++ks) {
      bo[ks] = *(const bf16x8*)(slot + (orow ^ (32 * ks)));
#pragma unroll
      for (int i = 0; i < 3; ++i) {
        const s16x4 lo = tr_read(slot + tra[i][0] + 2048 * ks), hi4 = tr_read(slot + tra[i][1] + 2048 * ks);
        aw[ks][i] = __builtin_shufflevector(lo, hi4, 0, 1, 2, 3, 4, 5, 6, 7);
      }
    }
    __builtin_amdgcn_sched_barrier(0);
#pragma unroll
    for (int ks = 0; ks < 4; ++ks)
#pragma unroll
      for (int i = 0; i < 3; ++i) acc[i] = __builtin_amdgcn_mfma_f32_32x32x16_bf16(aw[ks][i], bo[ks], acc[i], 0, 0, 0);
  }
  float* dst = part + (size_t)bz * (NB * SQ * DM) + (size_t)(bt * SQ + 32 * wm + r32) * DM + n0 + 96 * wn;
#pragma unroll
  for (int i = 0; i < 3; ++i)
#pragma unroll
    for (int g = 0; g < 4; ++g) {
      typedef float f32x4 __attribute__((ext_vector_type(4)));
      f32x4 v = {acc[i][4 * g], acc[i][4 * g + 1], acc[i][4 * g + 2], acc[i][4 * g + 3]};
      *(f32x4*)(dst + 32 * i + 8 * g + 4 * hi) = v;
    }
}

__global__ __launch_bounds__(256) void reduce_kernel(const float* __restrict__ part, __hip_bfloat16* __restrict__ out) {
  typedef float f32x4 __attribute__((ext_vector_type(4)));
  const size_t i = ((size_t)blockIdx.x * 256 + threadIdx.x) * 4;
  f32x4 a = *(const f32x4*)(part + i);
#pragma unroll
  for (int z = 1; z < K2_SPLIT; ++z) { const f32x4 t = *(const f32x4*)(part + (size_t)z * (NB * SQ * DM) + i); a += t; }
  u32x2 v = {cvtpk(a[0], a[1]), cvtpk(a[2], a[3])};
  *(u32x2*)((char*)out + i * 2) = v;
}

extern "C" void kernel_launch(void* const* d_in, const int* in_sizes, int n_in,
                              void* d_out, int out_size, void* d_ws, size_t ws_size,
                              hipStream_t stream) {
  const __hip_bfloat16* x  = (const __hip_bfloat16*)d_in[0];
  const __hip_bfloat16* kc = (const __hip_bfloat16*)d_in[1];
  const __hip_bfloat16* vc = (const __hip_bfloat16*)d_in[2];
  const __hip_bfloat16* wq = (const __hip_bfloat16*)d_in[3];
  const __hip_bfloat16* wo = (const __hip_bfloat16*)d_in[4];
  __hip_bfloat16* obuf = (__hip_bfloat16*)d_ws;
  __hip_bfloat16* out = (__hip_bfloat16*)d_out;
  float* part = (float*)((char*)d_ws + (size_t)NB * SQ * HD * 2);
  hipFuncSetAttribute((const void*)attn_fused_kernel, hipFuncAttributeMaxDynamicSharedMemorySize, K1_LDS);
  hipLaunchKernelGGL(attn_fused_kernel, dim3(NB * NH), dim3(512), K1_LDS, stream, x, kc, vc, wq, obuf);
  hipFuncSetAttribute((const void*)outproj_kernel, hipFuncAttributeMaxDynamicSharedMemorySize, K2_NS * K2_STAGE);
  hipLaunchKernelGGL(outproj_kernel, dim3(8 * 3 * K2_SPLIT), dim3(512), K2_NS * K2_STAGE, stream, (const __hip_bfloat16*)obuf, wo, part);
  hipLaunchKernelGGL(reduce_kernel, dim3(NB * SQ * DM / 1024), dim3(256), 0, stream, (const float*)part, out);
}
```

```cpp
#include <hip/hip_runtime.h>
#include <hip/hip_bf16.h>

typedef short    bf16x8 __attribute__((ext_vector_type(8)));
typedef short    s16x4  __attribute__((ext_vector_type(4)));
typedef float    f32x16 __attribute__((ext_vector_type(16)));
typedef float    f32x2  __attribute__((ext_vector_type(2)));
typedef __bf16   bf16x2 __attribute__((ext_vector_type(2)));
typedef unsigned u32x4  __attribute__((ext_vector_type(4)));
typedef unsigned u32x2  __attribute__((ext_vector_type(2)));
typedef __attribute__((address_space(3))) s16x4 lds_s16x4;

constexpr int NB = 8, SQ = 128, DM = 576, NH = 32, SK = 1024, DH = 128, HD = NH * DH;
constexpr int TILE_BYTES = 16384;
constexpr int PAIR_BYTES = 2 * TILE_BYTES;
constexpr int NP = 5;
constexpr int NPA = DM / 64;
constexpr int NPB = SK / 64;
constexpr int NPAIR = NPA + NPB;
constexpr int K1_LDS = NP * PAIR_BYTES;
constexpr float SCALE_L2E = 0.088388347648318447f * 1.4426950408889634f;

#define DI __device__ __forceinline__
DI unsigned cvtpk(float lo, float hi) { f32x2 v = {lo, hi}; bf16x2 b = __builtin_convertvector(v, bf16x2); return __builtin_bit_cast(unsigned, b); }
DI s16x4 tr_read(const char* p) { return __builtin_amdgcn_ds_read_tr16_b64_v4i16((lds_s16x4*)(p)); }
DI void glds_asm(const void* gsrc, const char* lds_dst) {
  unsigned keep; const unsigned d = __builtin_amdgcn_readfirstlane((unsigned)(size_t)lds_dst);
  asm volatile("s_mov_b32 %0, m0\n\ts_mov_b32 m0, %2\n\ts_nop 0\n\tglobal_load_lds_dwordx4 %1, off\n\ts_mov_b32 m0, %0" : "=&s"(keep) : "v"(gsrc), "s"(d) : "memory");
}
DI void glds_asm_nt(const void* gsrc, const char* lds_dst) {
  unsigned keep; const unsigned d = __builtin_amdgcn_readfirstlane((unsigned)(size_t)lds_dst);
  asm volatile("s_mov_b32 %0, m0\n\ts_mov_b32 m0, %2\n\ts_nop 0\n\tglobal_load_lds_dwordx4 %1, off nt\n\ts_mov_b32 m0, %0" : "=&s"(keep) : "v"(gsrc), "s"(d) : "memory");
}
#define GLDS(src, dst, aux) do { if ((aux) == 2) glds_asm_nt((src), (dst)); else glds_asm((src), (dst)); } while (0)

template <int N> DI void wait_vm() { asm volatile("s_waitcnt vmcnt(%0)" ::"n"(N) : "memory"); }

DI bf16x8 pack_nat(const f32x16& a, int base) {
  unsigned a0 = cvtpk(a[base + 0], a[base + 1]), a1 = cvtpk(a[base + 2], a[base + 3]);
  unsigned b0 = cvtpk(a[base + 4], a[base + 5]), b1 = cvtpk(a[base + 6], a[base + 7]);
  auto r0 = __builtin_amdgcn_permlane32_swap(a0, b0, false, false);
  auto r1 = __builtin_amdgcn_permlane32_swap(a1, b1, false, false);
  u32x4 w = {r0[0], r1[0], r0[1], r1[1]};
  return __builtin_bit_cast(bf16x8, w);
}
DI bf16x8 pack_raw(const f32x16& a, int base) {
  u32x4 w = {cvtpk(a[base + 0], a[base + 1]), cvtpk(a[base + 2], a[base + 3]), cvtpk(a[base + 4], a[base + 5]), cvtpk(a[base + 6], a[base + 7])};
  return __builtin_bit_cast(bf16x8, w);
}

__global__ __launch_bounds__(512, 2)
void attn_fused_kernel(const __hip_bfloat16* __restrict__ x, const __hip_bfloat16* __restrict__ kc, const __hip_bfloat16* __restrict__ vc,
                       const __hip_bfloat16* __restrict__ wq, __hip_bfloat16* __restrict__ obuf) {
  extern __shared__ __attribute__((aligned(16))) char shm[];
  const int tid = threadIdx.x, lane = tid & 63, r32 = lane & 31, hi = lane >> 5;
  const int w8 = __builtin_amdgcn_readfirstlane(tid >> 6);
  const int w = w8 & 3, grp = w8 >> 2;
  const int bh = blockIdx.x, b = bh >> 5, h = bh & 31;

  const int l4 = lane >> 4;
  const char* kbase = (const char*)kc + (size_t)bh * SK * DH * 2;
  const char* vbase = (const char*)vc + (size_t)bh * SK * DH * 2;
  const char* wbase = (const char*)wq + (size_t)h * DH * 2;
  const char* xbase = (const char*)x + (size_t)b * SQ * DM * 2;
  auto issue_pair = [&](int pi) {
    char* slot = shm + (pi % NP) * PAIR_BYTES;
#pragma unroll
    for (int ii = 0; ii < 2; ++ii) {
      const int j = w8 * 2 + ii;
      const int row = 4 * j + l4;
      const int ch = (lane & 15) ^ ((l4 << 2) | (j & 3));
      if (pi < NPA) {
        GLDS(wbase + (size_t)(pi * 64 + row) * (HD * 2) + ch * 16, slot + j * 1024, 0);
        const int xr = 8 * j + (lane >> 3);
        const int xc = (lane & 7) ^ ((j & 1) * 4 + l4);
        GLDS(xbase + (size_t)xr * (DM * 2) + pi * 128 + xc * 16, slot + TILE_BYTES + j * 1024, 0);
      } else {
        const int t = pi - NPA;
        GLDS(kbase + (size_t)t * TILE_BYTES + row * 256 + ch * 16, slot + j * 1024, 2);
        GLDS(vbase + (size_t)t * TILE_BYTES + row * 256 + ch * 16, slot + TILE_BYTES + j * 1024, 2);
      }
    }
  };
  int nxt = 0;
  for (; nxt < NP - 1; ++nxt) issue_pair(nxt);

  const int q4 = (lane & 15) >> 2, p4 = lane & 3, blk = (lane >> 4) & 1;
  int trn[2][2], trp[2][4];
#pragma unroll
  for (int t = 0; t < 2; ++t) {
#pragma unroll
    for (int c = 0; c < 4; ++c) {
      const int ch = 4 * c + 2 * blk + (p4 >> 1);
      const int row = 8 * t + 4 * hi + q4; const int sw = ((row & 3) << 2) | ((row >> 2) & 3);
      trp[t][c] = 256 * row + 16 * (ch ^ sw) + 8 * (p4 & 1);
    }
#pragma unroll
    for (int cc = 0; cc < 2; ++cc) {
      const int ch = 4 * (2 * grp + cc) + 2 * blk + (p4 >> 1);
      const int row = 8 * hi + 4 * t + q4; const int sw = ((row & 3) << 2) | ((row >> 2) & 3);
      trn[t][cc] = 256 * row + 16 * (ch ^ sw) + 8 * (p4 & 1);
    }
  }
  const int swr = ((r32 & 3) << 2) | ((r32 >> 2) & 3);
  const int krow = 256 * r32 + 16 * (hi ^ swr);
  const int xq = 32 * w + r32;
  const int xrow = 128 * xq + 16 * (hi ^ ((xq >> 1) & 7));

  f32x16 acc[2] = {f32x16{}, f32x16{}};
  for (int pi = 0; pi < NPA; ++pi) {
    wait_vm<4 * (NP - 2)>();
    asm volatile("s_waitcnt lgkmcnt(0)" ::: "memory");
    __builtin_amdgcn_s_barrier();
    if (nxt < NPAIR) { issue_pair(nxt); ++nxt; }
    const char* slot = shm + (pi % NP) * PAIR_BYTES;
    bf16x8 bx[4], aw[4][2];
#pragma unroll
    for (int ks = 0; ks < 4; ++ks) {
      bx[ks] = *(const bf16x8*)(slot + TILE_BYTES + (xrow ^ (32 * ks)));
#pragma unroll
      for (int cc = 0; cc < 2; ++cc) {
        const s16x4 lo = tr_read(slot + trn[0][cc] + 4096 * ks), hi4 = tr_read(slot + trn[1][cc] + 4096 * ks);
        aw[ks][cc] = __builtin_shufflevector(lo, hi4, 0, 1, 2, 3, 4, 5, 6, 7);
      }
    }
    __builtin_amdgcn_sched_barrier(0);
#pragma unroll
    for (int ks = 0; ks < 4; ++ks)
#pragma unroll
      for (int cc = 0; cc < 2; ++cc) acc[cc] = __builtin_amdgcn_mfma_f32_32x32x16_bf16(aw[ks][cc], bx[ks], acc[cc], 0, 0, 0);
  }
  bf16x8 qf[8];
  {
    char* xch = shm + ((NPA - 1) % NP) * PAIR_BYTES;
    asm volatile("s_waitcnt lgkmcnt(0)" ::: "memory");
    __builtin_amdgcn_s_barrier();
#pragma unroll
    for (int cc = 0; cc < 2; ++cc) {
      qf[4 * grp + 2 * cc] = pack_nat(acc[cc], 0); qf[4 * grp + 2 * cc + 1] = pack_nat(acc[cc], 8);
      *(bf16x8*)(xch + ((w * 8 + 4 * grp + 2 * cc) * 64 + lane) * 16) = qf[4 * grp + 2 * cc];
      *(bf16x8*)(xch + ((w * 8 + 4 * grp + 2 * cc + 1) * 64 + lane) * 16) = qf[4 * grp + 2 * cc + 1];
    }
    asm volatile("s_waitcnt lgkmcnt(0)" ::: "memory");
    __builtin_amdgcn_s_barrier();
#pragma unroll
    for (int s = 0; s < 8; ++s) qf[s] = *(const bf16x8*)(xch + ((w * 8 + s) * 64 + lane) * 16);
  }

  f32x16 o[4];
#pragma unroll
  for (int c = 0; c < 4; ++c) o[c] = f32x16{};
  float m_run = -__builtin_inff(), l_run = 0.f;
  for (int P = NPA; P < NPAIR; P += 2) {
    if (P + 2 < NPAIR) wait_vm<4>(); else wait_vm<0>();
    asm volatile("s_waitcnt lgkmcnt(0)" ::: "memory");
    __builtin_amdgcn_s_barrier();
    while (nxt < NPAIR && nxt < P + NP) { issue_pair(nxt); ++nxt; }
    const char* slot = shm + ((P + grp) % NP) * PAIR_BYTES;
    f32x16 p0 = f32x16{}, p1 = f32x16{};
    bf16x8 kf0[8], kf1[8];
#pragma unroll
    for (int s = 0; s < 8; ++s) {
      kf0[s] = *(const bf16x8*)(slot + (krow ^ (32 * s)));
      kf1[s] = *(const bf16x8*)(slot + 8192 + (krow ^ (32 * s)));
    }
    __builtin_amdgcn_sched_barrier(0);
#pragma unroll
    for (int s = 0; s < 8; ++s) {
      p0 = __builtin_amdgcn_mfma_f32_32x32x16_bf16(kf0[s], qf[s], p0, 0, 0, 0);
      p1 = __builtin_amdgcn_mfma_f32_32x32x16_bf16(kf1[s], qf[s], p1, 0, 0, 0);
    }
    __builtin_amdgcn_sched_barrier(0);
    bf16x8 av[4][4];
    { const char* vs = slot + TILE_BYTES;
#pragma unroll
      for (int c = 0; c < 2; ++c)
#pragma unroll
        for (int ks = 0; ks < 4; ++ks) {
          const s16x4 lo = tr_read(vs + trp[0][c] + 4096 * ks), hi4 = tr_read(vs + trp[1][c] + 4096 * ks);
          av[c][ks] = __builtin_shufflevector(lo, hi4, 0, 1, 2, 3, 4, 5, 6, 7);
        } }
    __builtin_amdgcn_sched_barrier(0);
    float mx = p0[0];
#pragma unroll
    for (int r = 1; r < 16; ++r) mx = fmaxf(mx, p0[r]);
#pragma unroll
    for (int r = 0; r < 16; ++r) mx = fmaxf(mx, p1[r]);
    { auto rr = __builtin_amdgcn_permlane32_swap(__float_as_uint(mx), __float_as_uint(mx), false, false);
      mx = fmaxf(__uint_as_float(rr[0]), __uint_as_float(rr[1])); }
    const float m_new = fmaxf(m_run, mx);
    if (__any(m_new > m_run)) {
      const float alpha = __builtin_amdgcn_exp2f((m_run - m_new) * SCALE_L2E);
#pragma unroll
      for (int c = 0; c < 4; ++c)
#pragma unroll
        for (int r = 0; r < 16; ++r) o[c][r] *= alpha;
      l_run *= alpha;
      m_run = m_new;
    }
    const float nm = -m_run * SCALE_L2E;
    float ps = 0.f;
#pragma unroll
    for (int r = 0; r < 16; ++r) { p0[r] = __builtin_amdgcn_exp2f(fmaf(p0[r], SCALE_L2E, nm)); ps += p0[r]; }
#pragma unroll
    for (int r = 0; r < 16; ++r) { p1[r] = __builtin_amdgcn_exp2f(fmaf(p1[r], SCALE_L2E, nm)); ps += p1[r]; }
    l_run += ps;
    bf16x8 pb[4];
    pb[0] = pack_raw(p0, 0); pb[1] = pack_raw(p0, 8); pb[2] = pack_raw(p1, 0); pb[3] = pack_raw(p1, 8);
    __builtin_amdgcn_sched_barrier(0);
    { const char* vs = slot + TILE_BYTES;
#pragma unroll
      for (int c = 2; c < 4; ++c)
#pragma unroll
        for (int ks = 0; ks < 4; ++ks) {
          const s16x4 lo = tr_read(vs + trp[0][c] + 4096 * ks), hi4 = tr_read(vs + trp[1][c] + 4096 * ks);
          av[c][ks] = __builtin_shufflevector(lo, hi4, 0, 1, 2, 3, 4, 5, 6, 7);
        } }
    __builtin_amdgcn_sched_barrier(0);
#pragma unroll
    for (int c = 0; c < 4; ++c)
#pragma unroll
      for (int ks = 0; ks < 4; ++ks) o[c] = __builtin_amdgcn_mfma_f32_32x32x16_bf16(av[c][ks], pb[ks], o[c], 0, 0, 0);
  }
  { auto rr = __builtin_amdgcn_permlane32_swap(__float_as_uint(l_run), __float_as_uint(l_run), false, false);
    l_run = __uint_as_float(rr[0]) + __uint_as_float(rr[1]); }
  asm volatile("s_waitcnt lgkmcnt(0)" ::: "memory");
  __builtin_amdgcn_s_barrier();
  float* mg = (float*)shm + w * (66 * 64);
  if (grp == 1) {
#pragma unroll
    for (int c = 0; c < 4; ++c)
#pragma unroll
      for (int r = 0; r < 16; ++r) mg[(c * 16 + r) * 64 + lane] = o[c][r];
    mg[64 * 64 + lane] = m_run; mg[65 * 64 + lane] = l_run;
  }
  asm volatile("s_waitcnt lgkmcnt(0)" ::: "memory");
  __builtin_amdgcn_s_barrier();
  if (grp == 0) {
    const float m1 = mg[64 * 64 + lane], l1 = mg[65 * 64 + lane];
    const float mm = fmaxf(m_run, m1);
    const float a0 = __builtin_amdgcn_exp2f((m_run - mm) * SCALE_L2E), a1 = __builtin_amdgcn_exp2f((m1 - mm) * SCALE_L2E);
    const float inv = 1.f / (l_run * a0 + l1 * a1);
    const float s0 = a0 * inv, s1 = a1 * inv;
    char* orow = (char*)obuf + ((size_t)bh * SQ + 32 * w + r32) * (DH * 2);
#pragma unroll
    for (int c = 0; c < 4; ++c)
#pragma unroll
      for (int g = 0; g < 4; ++g) {
        float v[4];
#pragma unroll
        for (int e = 0; e < 4; ++e) v[e] = o[c][4 * g + e] * s0 + mg[(c * 16 + 4 * g + e) * 64 + lane] * s1;
        u32x2 pk = {cvtpk(v[0], v[1]), cvtpk(v[2], v[3])};
        *(u32x2*)(orow + (32 * c + 8 * g + 4 * hi) * 2) = pk;
      }
  }
}

#define K2NS 3
constexpr int K2_KT = 64, K2_NS = K2NS, K2_SPLIT = 8, K2_STAGE = 40960;
__global__ __launch_bounds__(512, 2)
void outproj_kernel(const __hip_bfloat16* __restrict__ obuf, const __hip_bfloat16* __restrict__ wo, float* __restrict__ part) {
  extern __shared__ __attribute__((aligned(16))) char shm[];
  const int tid = threadIdx.x, lane = tid & 63, r32 = lane & 31, hi = lane >> 5;
  const int w8 = __builtin_amdgcn_readfirstlane(tid >> 6);
  const int wm = w8 & 3, wn = w8 >> 2;
  const int bz = blockIdx.x & 7, tile = blockIdx.x >> 3;
  const int bt = tile & 7, n0 = (tile >> 3) * 192;
  const int l3 = lane >> 3;
  constexpr int NKT = HD / K2_KT / K2_SPLIT;
  auto issue = [&](int kt, int buf) {
    kt += bz * NKT;
    char* slot = shm + buf * K2_STAGE;
#pragma unroll
    for (int i = 0; i < 5; ++i) {
      const int id = w8 * 5 + i, st = id >> 3, pc = id & 7;
      const int row = pc * 8 + l3;
      const int ch = (lane & 7) ^ ((row >> 1) & 7);
      if (st < 3) GLDS((const char*)wo + ((size_t)(kt * 64 + row) * DM + n0 + st * 64) * 2 + ch * 16, slot + id * 1024, 0);
      else GLDS((const char*)obuf + ((size_t)(bt * NH + (kt >> 1)) * SQ + (st - 3) * 64 + row) * (DH * 2) + (kt & 1) * 128 + ch * 16, slot + id * 1024, 0);
    }
  };
  const int q4 = (lane & 15) >> 2, p4 = lane & 3, blk = (lane >> 4) & 1;
  int tra[3][2];
#pragma unroll
  for (int i = 0; i < 3; ++i)
#pragma unroll
    for (int t = 0; t < 2; ++t) {
      const int nb = 3 * wn + i;
      const int row = 8 * hi + 4 * t + q4;
      const int ch = 4 * (nb & 1) + 2 * blk + (p4 >> 1);
      tra[i][t] = (nb >> 1) * 8192 + 128 * row + 16 * (ch ^ ((row >> 1) & 7)) + 8 * (p4 & 1);
    }
  const int mr = 32 * (wm & 1) + r32;
  const int orow = (3 + (wm >> 1)) * 8192 + 128 * mr + 16 * (hi ^ ((mr >> 1) & 7));
  f32x16 acc[3] = {f32x16{}, f32x16{}, f32x16{}};
#pragma unroll
  for (int i = 0; i < K2_NS - 1; ++i) issue(i, i);
  for (int kt = 0; kt < NKT; ++kt) {
    const int buf = kt % K2_NS;
    if (kt + K2_NS - 2 < NKT) wait_vm<5 * (K2_NS - 2)>(); else wait_vm<0>();
    asm volatile("s_waitcnt lgkmcnt(0)" ::: "memory");
    __builtin_amdgcn_s_barrier();
    if (kt + K2_NS - 1 < NKT) issue(kt + K2_NS - 1, (kt + K2_NS - 1) % K2_NS);
    const char* slot = shm + buf * K2_STAGE;
    bf16x8 bo[4], aw[4][3];
#pragma unroll
    for (int ks = 0; ks < 4; ++ks) {
      bo[ks] = *(const bf16x8*)(slot + (orow ^ (32 * ks)));
#pragma unroll
      for (int i = 0; i < 3; ++i) {
        const s16x4 lo = tr_read(slot + tra[i][0] + 2048 * ks), hi4 = tr_read(slot + tra[i][1] + 2048 * ks);
        aw[ks][i] = __builtin_shufflevector(lo, hi4, 0, 1, 2, 3, 4, 5, 6, 7);
      }
    }
    __builtin_amdgcn_sched_barrier(0);
#pragma unroll
    for (int ks = 0; ks < 4; ++ks)
#pragma unroll
      for (int i = 0; i < 3; ++i) acc[i] = __builtin_amdgcn_mfma_f32_32x32x16_bf16(aw[ks][i], bo[ks], acc[i], 0, 0, 0);
  }
  char* dst = (char*)part + ((size_t)bz * (NB * SQ * DM) + (size_t)(bt * SQ + 32 * wm + r32) * DM + n0 + 96 * wn) * 2;
#pragma unroll
  for (int i = 0; i < 3; ++i)
#pragma unroll
    for (int g = 0; g < 4; ++g) {
      u32x2 v = {cvtpk(acc[i][4 * g], acc[i][4 * g + 1]), cvtpk(acc[i][4 * g + 2], acc[i][4 * g + 3])};
      *(u32x2*)(dst + (32 * i + 8 * g + 4 * hi) * 2) = v;
    }
}

__global__ __launch_bounds__(256) void reduce_kernel(const float* __restrict__ part, __hip_bfloat16* __restrict__ out) {
  const size_t i = ((size_t)blockIdx.x * 256 + threadIdx.x) * 8;
  float a[8] = {0.f, 0.f, 0.f, 0.f, 0.f, 0.f, 0.f, 0.f};
#pragma unroll
  for (int z = 0; z < K2_SPLIT; ++z) {
    const u32x4 t = *(const u32x4*)((const char*)part + ((size_t)z * (NB * SQ * DM) + i) * 2);
#pragma unroll
    for (int e = 0; e < 4; ++e) { a[2 * e] += __uint_as_float(t[e] << 16); a[2 * e + 1] += __uint_as_float(t[e] & 0xffff0000u); }
  }
  u32x4 v = {cvtpk(a[0], a[1]), cvtpk(a[2], a[3]), cvtpk(a[4], a[5]), cvtpk(a[6], a[7])};
  *(u32x4*)((char*)out + i * 2) = v;
}

extern "C" void kernel_launch(void* const* d_in, const int* in_sizes, int n_in,
                              void* d_out, int out_size, void* d_ws, size_t ws_size,
                              hipStream_t stream) {
  const __hip_bfloat16* x  = (const __hip_bfloat16*)d_in[0];
  const __hip_bfloat16* kc = (const __hip_bfloat16*)d_in[1];
  const __hip_bfloat16* vc = (const __hip_bfloat16*)d_in[2];
  const __hip_bfloat16* wq = (const __hip_bfloat16*)d_in[3];
  const __hip_bfloat16* wo = (const __hip_bfloat16*)d_in[4];
  __hip_bfloat16* obuf = (__hip_bfloat16*)d_ws;
  __hip_bfloat16* out = (__hip_bfloat16*)d_out;
  float* part = (float*)((char*)d_ws + (size_t)NB * SQ * HD * 2);
  hipFuncSetAttribute((const void*)attn_fused_kernel, hipFuncAttributeMaxDynamicSharedMemorySize, K1_LDS);
  hipLaunchKernelGGL(attn_fused_kernel, dim3(NB * NH), dim3(512), K1_LDS, stream, x, kc, vc, wq, obuf);
  hipFuncSetAttribute((const void*)outproj_kernel, hipFuncAttributeMaxDynamicSharedMemorySize, K2_NS * K2_STAGE);
  hipLaunchKernelGGL(outproj_kernel, dim3(8 * 3 * K2_SPLIT), dim3(512), K2_NS * K2_STAGE, stream, (const __hip_bfloat16*)obuf, wo, part);
  hipLaunchKernelGGL(reduce_kernel, dim3(NB * SQ * DM / 2048), dim3(256), 0, stream, (const float*)part, out);
}
```

```cpp
#include <hip/hip_runtime.h>
#include <hip/hip_bf16.h>

typedef short    bf16x8 __attribute__((ext_vector_type(8)));
typedef short    s16x4  __attribute__((ext_vector_type(4)));
typedef float    f32x16 __attribute__((ext_vector_type(16)));
typedef float    f32x2  __attribute__((ext_vector_type(2)));
typedef __bf16   bf16x2 __attribute__((ext_vector_type(2)));
typedef unsigned u32x4  __attribute__((ext_vector_type(4)));
typedef unsigned u32x2  __attribute__((ext_vector_type(2)));
typedef __attribute__((address_space(3))) s16x4 lds_s16x4;

constexpr int NB = 8, SQ = 128, DM = 576, NH = 32, SK = 1024, DH = 128, HD = NH * DH;
constexpr int TILE_BYTES = 16384;
constexpr int PAIR_BYTES = 2 * TILE_BYTES;
constexpr int NP = 5;
constexpr int NPA = DM / 64;
constexpr int NPB = SK / 64;
constexpr int NPAIR = NPA + NPB;
constexpr int K1_LDS = NP * PAIR_BYTES;
constexpr float SCALE_L2E = 0.088388347648318447f * 1.4426950408889634f;

#define DI __device__ __forceinline__
DI unsigned cvtpk(float lo, float hi) { f32x2 v = {lo, hi}; bf16x2 b = __builtin_convertvector(v, bf16x2); return __builtin_bit_cast(unsigned, b); }
DI s16x4 tr_read(const char* p) { return __builtin_amdgcn_ds_read_tr16_b64_v4i16((lds_s16x4*)(p)); }
DI void glds_asm(const void* gsrc, const char* lds_dst) {
  unsigned keep; const unsigned d = __builtin_amdgcn_readfirstlane((unsigned)(size_t)lds_dst);
  asm volatile("s_mov_b32 %0, m0\n\ts_mov_b32 m0, %2\n\ts_nop 0\n\tglobal_load_lds_dwordx4 %1, off\n\ts_mov_b32 m0, %0" : "=&s"(keep) : "v"(gsrc), "s"(d) : "memory");
}
DI void glds_asm_nt(const void* gsrc, const char* lds_dst) {
  unsigned keep; const unsigned d = __builtin_amdgcn_readfirstlane((unsigned)(size_t)lds_dst);
  asm volatile("s_mov_b32 %0, m0\n\ts_mov_b32 m0, %2\n\ts_nop 0\n\tglobal_load_lds_dwordx4 %1, off nt\n\ts_mov_b32 m0, %0" : "=&s"(keep) : "v"(gsrc), "s"(d) : "memory");
}
#define GLDS(src, dst, aux) do { if ((aux) == 2) glds_asm_nt((src), (dst)); else glds_asm((src), (dst)); } while (0)

template <int N> DI void wait_vm() { asm volatile("s_waitcnt vmcnt(%0)" ::"n"(N) : "memory"); }

DI bf16x8 pack_nat(const f32x16& a, int base) {
  unsigned a0 = cvtpk(a[base + 0], a[base + 1]), a1 = cvtpk(a[base + 2], a[base + 3]);
  unsigned b0 = cvtpk(a[base + 4], a[base + 5]), b1 = cvtpk(a[base + 6], a[base + 7]);
  auto r0 = __builtin_amdgcn_permlane32_swap(a0, b0, false, false);
  auto r1 = __builtin_amdgcn_permlane32_swap(a1, b1, false, false);
  u32x4 w = {r0[0], r1[0], r0[1], r1[1]};
  return __builtin_bit_cast(bf16x8, w);
}
DI bf16x8 pack_raw(const f32x16& a, int base) {
  u32x4 w = {cvtpk(a[base + 0], a[base + 1]), cvtpk(a[base + 2], a[base + 3]), cvtpk(a[base + 4], a[base + 5]), cvtpk(a[base + 6], a[base + 7])};
  return __builtin_bit_cast(bf16x8, w);
}

__global__ __launch_bounds__(512, 2)
void attn_fused_kernel(const __hip_bfloat16* __restrict__ x, const __hip_bfloat16* __restrict__ kc, const __hip_bfloat16* __restrict__ vc,
                       const __hip_bfloat16* __restrict__ wq, __hip_bfloat16* __restrict__ obuf) {
  extern __shared__ __attribute__((aligned(16))) char shm[];
  const int tid = threadIdx.x, lane = tid & 63, r32 = lane & 31, hi = lane >> 5;
  const int w8 = __builtin_amdgcn_readfirstlane(tid >> 6);
  const int w = w8 & 3, grp = w8 >> 2;
  const int bh = blockIdx.x, b = bh >> 5, h = bh & 31;

  const int l4 = lane >> 4;
  const char* kbase = (const char*)kc + (size_t)bh * SK * DH * 2;
  const char* vbase = (const char*)vc + (size_t)bh * SK * DH * 2;
  const char* wbase = (const char*)wq + (size_t)h * DH * 2;
  const char* xbase = (const char*)x + (size_t)b * SQ * DM * 2;
  auto issue_wx = [&](int pi) {
    char* slot = shm + (2 + pi % 3) * PAIR_BYTES;
#pragma unroll
    for (int ii = 0; ii < 4; ++ii) {
      const int j = w * 4 + ii;
      const int row = 4 * j + l4;
      const int ch = (lane & 15) ^ ((l4 << 2) | (j & 3));
      GLDS(wbase + (size_t)(pi * 64 + row) * (HD * 2) + ch * 16, slot + j * 1024, 0);
      const int xr = 8 * j + (lane >> 3);
      const int xc = (lane & 7) ^ ((j & 1) * 4 + l4);
      GLDS(xbase + (size_t)xr * (DM * 2) + pi * 128 + xc * 16, slot + TILE_BYTES + j * 1024, 0);
    }
  };
  auto issue_kv = [&](int t) {
    char* slot = shm + (t % NP) * PAIR_BYTES;
#pragma unroll
    for (int ii = 0; ii < 4; ++ii) {
      const int j = w * 4 + ii;
      const int row = 4 * j + l4;
      const int ch = (lane & 15) ^ ((l4 << 2) | (j & 3));
      GLDS(kbase + (size_t)t * TILE_BYTES + row * 256 + ch * 16, slot + j * 1024, 2);
      GLDS(vbase + (size_t)t * TILE_BYTES + row * 256 + ch * 16, slot + TILE_BYTES + j * 1024, 2);
    }
  };
  int nxt = 2;
  if (grp == 0) { issue_wx(0); issue_wx(1); } else { issue_kv(0); issue_kv(1); }

  const int q4 = (lane & 15) >> 2, p4 = lane & 3, blk = (lane >> 4) & 1;
  int trn[2][2], trp[2][4];
#pragma unroll
  for (int t = 0; t < 2; ++t) {
#pragma unroll
    for (int c = 0; c < 4; ++c) {
      const int ch = 4 * c + 2 * blk + (p4 >> 1);
      const int row = 8 * t + 4 * hi + q4; const int sw = ((row & 3) << 2) | ((row >> 2) & 3);
      trp[t][c] = 256 * row + 16 * (ch ^ sw) + 8 * (p4 & 1);
    }
#pragma unroll
    for (int cc = 0; cc < 2; ++cc) {
      const int ch = 4 * (2 * grp + cc) + 2 * blk + (p4 >> 1);
      const int row = 8 * hi + 4 * t + q4; const int sw = ((row & 3) << 2) | ((row >> 2) & 3);
      trn[t][cc] = 256 * row + 16 * (ch ^ sw) + 8 * (p4 & 1);
    }
  }
  const int swr = ((r32 & 3) << 2) | ((r32 >> 2) & 3);
  const int krow = 256 * r32 + 16 * (hi ^ swr);
  const int xq = 32 * w + r32;
  const int xrow = 128 * xq + 16 * (hi ^ ((xq >> 1) & 7));

  f32x16 acc[2] = {f32x16{}, f32x16{}};
  for (int pi = 0; pi < NPA; ++pi) {
    if (grp == 0) { if (pi + 1 < NPA) wait_vm<8>(); else wait_vm<0>(); }
    asm volatile("s_waitcnt lgkmcnt(0)" ::: "memory");
    __builtin_amdgcn_s_barrier();
    if (grp == 0 && pi + 2 < NPA) issue_wx(pi + 2);
    const char* slot = shm + (2 + pi % 3) * PAIR_BYTES;
    bf16x8 bx[4], aw[4][2];
#pragma unroll
    for (int ks = 0; ks < 4; ++ks) {
      bx[ks] = *(const bf16x8*)(slot + TILE_BYTES + (xrow ^ (32 * ks)));
#pragma unroll
      for (int cc = 0; cc < 2; ++cc) {
        const s16x4 lo = tr_read(slot + trn[0][cc] + 4096 * ks), hi4 = tr_read(slot + trn[1][cc] + 4096 * ks);
        aw[ks][cc] = __builtin_shufflevector(lo, hi4, 0, 1, 2, 3, 4, 5, 6, 7);
      }
    }
    __builtin_amdgcn_sched_barrier(0);
#pragma unroll
    for (int ks = 0; ks < 4; ++ks)
#pragma unroll
      for (int cc = 0; cc < 2; ++cc) acc[cc] = __builtin_amdgcn_mfma_f32_32x32x16_bf16(aw[ks][cc], bx[ks], acc[cc], 0, 0, 0);
  }
  bf16x8 qf[8];
  {
    char* xch = shm + (2 + (NPA - 2) % 3) * PAIR_BYTES;
    asm volatile("s_waitcnt lgkmcnt(0)" ::: "memory");
    __builtin_amdgcn_s_barrier();
#pragma unroll
    for (int cc = 0; cc < 2; ++cc) {
      qf[4 * grp + 2 * cc] = pack_nat(acc[cc], 0); qf[4 * grp + 2 * cc + 1] = pack_nat(acc[cc], 8);
      *(bf16x8*)(xch + ((w * 8 + 4 * grp + 2 * cc) * 64 + lane) * 16) = qf[4 * grp + 2 * cc];
      *(bf16x8*)(xch + ((w * 8 + 4 * grp + 2 * cc + 1) * 64 + lane) * 16) = qf[4 * grp + 2 * cc + 1];
    }
    asm volatile("s_waitcnt lgkmcnt(0)" ::: "memory");
    __builtin_amdgcn_s_barrier();
#pragma unroll
    for (int s = 0; s < 8; ++s) qf[s] = *(const bf16x8*)(xch + ((w * 8 + s) * 64 + lane) * 16);
  }

  f32x16 o[4];
#pragma unroll
  for (int c = 0; c < 4; ++c) o[c] = f32x16{};
  float m_run = -__builtin_inff(), l_run = 0.f;
  for (int P = 0; P < NPB; P += 2) {
    if (grp == 1) { if (P > 0 && P + 2 < NPB) wait_vm<8>(); else wait_vm<0>(); }
    asm volatile("s_waitcnt lgkmcnt(0)" ::: "memory");
    __builtin_amdgcn_s_barrier();
    while (nxt < NPB && nxt < P + NP) { if (grp == 1) issue_kv(nxt); ++nxt; }
    const char* slot = shm + ((P + grp) % NP) * PAIR_BYTES;
    f32x16 p0 = f32x16{}, p1 = f32x16{};
    bf16x8 kf0[8], kf1[8];
#pragma unroll
    for (int s = 0; s < 8; ++s) {
      kf0[s] = *(const bf16x8*)(slot + (krow ^ (32 * s)));
      kf1[s] = *(const bf16x8*)(slot + 8192 + (krow ^ (32 * s)));
    }
    __builtin_amdgcn_sched_barrier(0);
#pragma unroll
    for (int s = 0; s < 8; ++s) {
      p0 = __builtin_amdgcn_mfma_f32_32x32x16_bf16(kf0[s], qf[s], p0, 0, 0, 0);
      p1 = __builtin_amdgcn_mfma_f32_32x32x16_bf16(kf1[s], qf[s], p1, 0, 0, 0);
    }
    __builtin_amdgcn_sched_barrier(0);
    bf16x8 av[4][4];
    { const char* vs = slot + TILE_BYTES;
#pragma unroll
      for (int c = 0; c < 2; ++c)
#pragma unroll
        for (int ks = 0; ks < 4; ++ks) {
          const s16x4 lo = tr_read(vs + trp[0][c] + 4096 * ks), hi4 = tr_read(vs + trp[1][c] + 4096 * ks);
          av[c][ks] = __builtin_shufflevector(lo, hi4, 0, 1, 2, 3, 4, 5, 6, 7);
        } }
    __builtin_amdgcn_sched_barrier(0);
    float mx = p0[0];
#pragma unroll
    for (int r = 1; r < 16; ++r) mx = fmaxf(mx, p0[r]);
#pragma unroll
    for (int r = 0; r < 16; ++r) mx = fmaxf(mx, p1[r]);
    { auto rr = __builtin_amdgcn_permlane32_swap(__float_as_uint(mx), __float_as_uint(mx), false, false);
      mx = fmaxf(__uint_as_float(rr[0]), __uint_as_float(rr[1])); }
    const float m_new = fmaxf(m_run, mx);
    if (__any(m_new > m_run)) {
      const float alpha = __builtin_amdgcn_exp2f((m_run - m_new) * SCALE_L2E);
#pragma unroll
      for (int c = 0; c < 4; ++c)
#pragma unroll
        for (int r = 0; r < 16; ++r) o[c][r] *= alpha;
      l_run *= alpha;
      m_run = m_new;
    }
    const float nm = -m_run * SCALE_L2E;
    float ps = 0.f;
#pragma unroll
    for (int r = 0; r < 16; ++r) { p0[r] = __builtin_amdgcn_exp2f(fmaf(p0[r], SCALE_L2E, nm)); ps += p0[r]; }
#pragma unroll
    for (int r = 0; r < 16; ++r) { p1[r] = __builtin_amdgcn_exp2f(fmaf(p1[r], SCALE_L2E, nm)); ps += p1[r]; }
    l_run += ps;
    bf16x8 pb[4];
    pb[0] = pack_raw(p0, 0); pb[1] = pack_raw(p0, 8); pb[2] = pack_raw(p1, 0); pb[3] = pack_raw(p1, 8);
    __builtin_amdgcn_sched_barrier(0);
    { const char* vs = slot + TILE_BYTES;
#pragma unroll
      for (int c = 2; c < 4; ++c)
#pragma unroll
        for (int ks = 0; ks < 4; ++ks) {
          const s16x4 lo = tr_read(vs + trp[0][c] + 4096 * ks), hi4 = tr_read(vs + trp[1][c] + 4096 * ks);
          av[c][ks] = __builtin_shufflevector(lo, hi4, 0, 1, 2, 3, 4, 5, 6, 7);
        } }
    __builtin_amdgcn_sched_barrier(0);
#pragma unroll
    for (int c = 0; c < 4; ++c)
#pragma unroll
      for (int ks = 0; ks < 4; ++ks) o[c] = __builtin_amdgcn_mfma_f32_32x32x16_bf16(av[c][ks], pb[ks], o[c], 0, 0, 0);
  }
  { auto rr = __builtin_amdgcn_permlane32_swap(__float_as_uint(l_run), __float_as_uint(l_run), false, false);
    l_run = __uint_as_float(rr[0]) + __uint_as_float(rr[1]); }
  asm volatile("s_waitcnt lgkmcnt(0)" ::: "memory");
  __builtin_amdgcn_s_barrier();
  float* mg = (float*)shm + w * (66 * 64);
  if (grp == 1) {
#pragma unroll
    for (int c = 0; c < 4; ++c)
#pragma unroll
      for (int r = 0; r < 16; ++r) mg[(c * 16 + r) * 64 + lane] = o[c][r];
    mg[64 * 64 + lane] = m_run; mg[65 * 64 + lane] = l_run;
  }
  asm volatile("s_waitcnt lgkmcnt(0)" ::: "memory");
  __builtin_amdgcn_s_barrier();
  if (grp == 0) {
    const float m1 = mg[64 * 64 + lane], l1 = mg[65 * 64 + lane];
    const float mm = fmaxf(m_run, m1);
    const float a0 = __builtin_amdgcn_exp2f((m_run - mm) * SCALE_L2E), a1 = __builtin_amdgcn_exp2f((m1 - mm) * SCALE_L2E);
    const float inv = 1.f / (l_run * a0 + l1 * a1);
    const float s0 = a0 * inv, s1 = a1 * inv;
    char* orow = (char*)obuf + ((size_t)bh * SQ + 32 * w + r32) * (DH * 2);
#pragma unroll
    for (int c = 0; c < 4; ++c)
#pragma unroll
      for (int g = 0; g < 4; ++g) {
        float v[4];
#pragma unroll
        for (int e = 0; e < 4; ++e) v[e] = o[c][4 * g + e] * s0 + mg[(c * 16 + 4 * g + e) * 64 + lane] * s1;
        u32x2 pk = {cvtpk(v[0], v[1]), cvtpk(v[2], v[3])};
        *(u32x2*)(orow + (32 * c + 8 * g + 4 * hi) * 2) = pk;
      }
  }
}

#define K2NS 3
constexpr int K2_KT = 64, K2_NS = K2NS, K2_SPLIT = 8, K2_STAGE = 40960;
__global__ __launch_bounds__(512, 2)
void outproj_kernel(const __hip_bfloat16* __restrict__ obuf, const __hip_bfloat16* __restrict__ wo, float* __restrict__ part) {
  extern __shared__ __attribute__((aligned(16))) char shm[];
  const int tid = threadIdx.x, lane = tid & 63, r32 = lane & 31, hi = lane >> 5;
  const int w8 = __builtin_amdgcn_readfirstlane(tid >> 6);
  const int wm = w8 & 3, wn = w8 >> 2;
  const int bz = blockIdx.x & 7, tile = blockIdx.x >> 3;
  const int bt = tile & 7, n0 = (tile >> 3) * 192;
  const int l3 = lane >> 3;
  constexpr int NKT = HD / K2_KT / K2_SPLIT;
  auto issue = [&](int kt, int buf) {
    kt += bz * NKT;
    char* slot = shm + buf * K2_STAGE;
#pragma unroll
    for (int i = 0; i < 5; ++i) {
      const int id = w8 * 5 + i, st = id >> 3, pc = id & 7;
      const int row = pc * 8 + l3;
      const int ch = (lane & 7) ^ ((row >> 1) & 7);
      if (st < 3) GLDS((const char*)wo + ((size_t)(kt * 64 + row) * DM + n0 + st * 64) * 2 + ch * 16, slot + id * 1024, 0);
      else GLDS((const char*)obuf + ((size_t)(bt * NH + (kt >> 1)) * SQ + (st - 3) * 64 + row) * (DH * 2) + (kt & 1) * 128 + ch * 16, slot + id * 1024, 0);
    }
  };
  const int q4 = (lane & 15) >> 2, p4 = lane & 3, blk = (lane >> 4) & 1;
  int tra[3][2];
#pragma unroll
  for (int i = 0; i < 3; ++i)
#pragma unroll
    for (int t = 0; t < 2; ++t) {
      const int nb = 3 * wn + i;
      const int row = 8 * hi + 4 * t + q4;
      const int ch = 4 * (nb & 1) + 2 * blk + (p4 >> 1);
      tra[i][t] = (nb >> 1) * 8192 + 128 * row + 16 * (ch ^ ((row >> 1) & 7)) + 8 * (p4 & 1);
    }
  const int mr = 32 * (wm & 1) + r32;
  const int orow = (3 + (wm >> 1)) * 8192 + 128 * mr + 16 * (hi ^ ((mr >> 1) & 7));
  f32x16 acc[3] = {f32x16{}, f32x16{}, f32x16{}};
#pragma unroll
  for (int i = 0; i < K2_NS - 1; ++i) issue(i, i);
  for (int kt = 0; kt < NKT; ++kt) {
    const int buf = kt % K2_NS;
    if (kt + K2_NS - 2 < NKT) wait_vm<5 * (K2_NS - 2)>(); else wait_vm<0>();
    asm volatile("s_waitcnt lgkmcnt(0)" ::: "memory");
    __builtin_amdgcn_s_barrier();
    if (kt + K2_NS - 1 < NKT) issue(kt + K2_NS - 1, (kt + K2_NS - 1) % K2_NS);
    const char* slot = shm + buf * K2_STAGE;
    bf16x8 bo[4], aw[4][3];
#pragma unroll
    for (int ks = 0; ks < 4; ++ks) {
      bo[ks] = *(const bf16x8*)(slot + (orow ^ (32 * ks)));
#pragma unroll
      for (int i = 0; i < 3; ++i) {
        const s16x4 lo = tr_read(slot + tra[i][0] + 2048 * ks), hi4 = tr_read(slot + tra[i][1] + 2048 * ks);
        aw[ks][i] = __builtin_shufflevector(lo, hi4, 0, 1, 2, 3, 4, 5, 6, 7);
      }
    }
    __builtin_amdgcn_sched_barrier(0);
#pragma unroll
    for (int ks = 0; ks < 4; ++ks)
#pragma unroll
      for (int i = 0; i < 3; ++i) acc[i] = __builtin_amdgcn_mfma_f32_32x32x16_bf16(aw[ks][i], bo[ks], acc[i], 0, 0, 0);
  }
  char* dst = (char*)part + ((size_t)bz * (NB * SQ * DM) + (size_t)(bt * SQ + 32 * wm + r32) * DM + n0 + 96 * wn) * 2;
#pragma unroll
  for (int i = 0; i < 3; ++i)
#pragma unroll
    for (int g = 0; g < 4; ++g) {
      u32x2 v = {cvtpk(acc[i][4 * g], acc[i][4 * g + 1]), cvtpk(acc[i][4 * g + 2], acc[i][4 * g + 3])};
      *(u32x2*)(dst + (32 * i + 8 * g + 4 * hi) * 2) = v;
    }
}

__global__ __launch_bounds__(256) void reduce_kernel(const float* __restrict__ part, __hip_bfloat16* __restrict__ out) {
  const size_t i = ((size_t)blockIdx.x * 256 + threadIdx.x) * 8;
  float a[8] = {0.f, 0.f, 0.f, 0.f, 0.f, 0.f, 0.f, 0.f};
#pragma unroll
  for (int z = 0; z < K2_SPLIT; ++z) {
    const u32x4 t = *(const u32x4*)((const char*)part + ((size_t)z * (NB * SQ * DM) + i) * 2);
#pragma unroll
    for (int e = 0; e < 4; ++e) { a[2 * e] += __uint_as_float(t[e] << 16); a[2 * e + 1] += __uint_as_float(t[e] & 0xffff0000u); }
  }
  u32x4 v = {cvtpk(a[0], a[1]), cvtpk(a[2], a[3]), cvtpk(a[4], a[5]), cvtpk(a[6], a[7])};
  *(u32x4*)((char*)out + i * 2) = v;
}

extern "C" void kernel_launch(void* const* d_in, const int* in_sizes, int n_in,
                              void* d_out, int out_size, void* d_ws, size_t ws_size,
                              hipStream_t stream) {
  const __hip_bfloat16* x  = (const __hip_bfloat16*)d_in[0];
  const __hip_bfloat16* kc = (const __hip_bfloat16*)d_in[1];
  const __hip_bfloat16* vc = (const __hip_bfloat16*)d_in[2];
  const __hip_bfloat16* wq = (const __hip_bfloat16*)d_in[3];
  const __hip_bfloat16* wo = (const __hip_bfloat16*)d_in[4];
  __hip_bfloat16* obuf = (__hip_bfloat16*)d_ws;
  __hip_bfloat16* out = (__hip_bfloat16*)d_out;
  float* part = (float*)((char*)d_ws + (size_t)NB * SQ * HD * 2);
  hipFuncSetAttribute((const void*)attn_fused_kernel, hipFuncAttributeMaxDynamicSharedMemorySize, K1_LDS);
  hipLaunchKernelGGL(attn_fused_kernel, dim3(NB * NH), dim3(512), K1_LDS, stream, x, kc, vc, wq, obuf);
  hipFuncSetAttribute((const void*)outproj_kernel, hipFuncAttributeMaxDynamicSharedMemorySize, K2_NS * K2_STAGE);
  hipLaunchKernelGGL(outproj_kernel, dim3(8 * 3 * K2_SPLIT), dim3(512), K2_NS * K2_STAGE, stream, (const __hip_bfloat16*)obuf, wo, part);
  hipLaunchKernelGGL(reduce_kernel, dim3(NB * SQ * DM / 2048), dim3(256), 0, stream, (const float*)part, out);
}
```
